```python
import torch

def _softmax(x, axis=-1):
    return torch.softmax(x, dim=axis)

def _gelu(x):
    return torch.nn.functional.gelu(x, approximate='tanh')

def _silu(x):
    return torch.nn.functional.silu(x)

def _sigmoid(x):
    return torch.sigmoid(x)

def _layernorm(x, axis=-1, eps=1e-05):
    m = x.mean(dim=axis, keepdim=True)
    v = x.var(dim=axis, keepdim=True, unbiased=False)
    return (x - m) / torch.sqrt(v + eps)

def _scatter_add(tgt, idx, src, axis=0):
    return tgt.clone().index_add_(axis, idx, src)

def _segment_sum(src, seg, n):
    out = torch.zeros((n,) + src.shape[1:], dtype=src.dtype)
    return out.index_add_(0, seg, src)

def _pad(x, pw):
    flat = tuple((v for pair in reversed(pw) for v in pair))
    return torch.nn.functional.pad(x, flat)
kernel_name = 'adaLN_cond__sliding_local_dilated__glu_gated_glu__prompt'

def setup_inputs():

    _w = lambda a: (lambda t: t.to(torch.bfloat16) if t.is_floating_point() else t)(torch.as_tensor(a))

    def _A_setup():
        B, S, D = (1, 2048, 512)
        return {'x': _w((lambda *s: torch.randn(*s, dtype=torch.bfloat16) * 0.05)(B, S, D)), 'wqkv': _w((lambda *s: torch.randn(*s, dtype=torch.bfloat16) * 0.05)(D, 3 * D)), 'wo': _w((lambda *s: torch.randn(*s, dtype=torch.bfloat16) * 0.05)(D, D))}
    _A_in = _A_setup()

    def _B_setup():
        B, S, D, F = (1, 2048, 512, 1376)
        return {'x': _w((lambda *s: torch.randn(*s, dtype=torch.bfloat16) * 0.05)(B, S, D)), 'w_gate': _w((lambda *s: torch.randn(*s, dtype=torch.bfloat16) * 0.05)(D, F)), 'w_up': _w((lambda *s: torch.randn(*s, dtype=torch.bfloat16) * 0.05)(D, F)), 'w_down': _w((lambda *s: torch.randn(*s, dtype=torch.bfloat16) * 0.05)(F, D))}
    _B_in = _B_setup()
    return {'A_x': _A_in['x'], 'A_wqkv': _A_in['wqkv'], 'A_wo': _A_in['wo'], 'B_x': _B_in['x'], 'B_w_gate': _B_in['w_gate'], 'B_w_up': _B_in['w_up'], 'B_w_down': _B_in['w_down'], 'c': _w((lambda *s: torch.randn(*s, dtype=torch.bfloat16) * 0.1)(1, 512) * 8.0), 'w_mod': _w((lambda *s: torch.randn(*s, dtype=torch.bfloat16) * 0.1)(512, 6 * 512) * 512 ** (-0.5) * 8.0)}

def reference(A_x, A_wqkv, A_wo, B_x, B_w_gate, B_w_up, B_w_down, c, w_mod):

    def _A_ref(x, wqkv, wo):
        B, S, D = x.shape
        H = 32
        Dh = D // H
        W, dil = (8, 2)
        qkv = (lambda x, *d: x.permute(*d))((x @ wqkv).reshape(B, S, 3, H, Dh), 2, 0, 3, 1, 4)
        q, k, v = (qkv[0], qkv[1], qkv[2])
        scale = Dh ** (-0.5)
        pos = torch.arange(S)
        off = torch.arange(W) * dil
        idx = pos[:, None] - off[None, :]
        mask = idx >= 0
        idx_c = torch.where(mask, idx, 0).reshape(-1)
        kg = k[:, :, idx_c, :].reshape(B, H, S, W, Dh)
        vg = v[:, :, idx_c, :].reshape(B, H, S, W, Dh)
        logits = torch.einsum('bhsd,bhswd->bhsw', q, kg) * scale
        logits = torch.where(mask[None, None, :, :], logits, -1000000000.0)
        attn = _softmax(logits, axis=-1)
        o = torch.einsum('bhsw,bhswd->bhsd', attn, vg)
        return (lambda x, *d: x.permute(*d))(o, 0, 2, 1, 3).reshape(B, S, D) @ wo

    def _B_ref(x, w_gate, w_up, w_down):
        g = torch.einsum('bsd,df->bsf', x, w_gate)
        u = torch.einsum('bsd,df->bsf', x, w_up)
        h = _sigmoid(g) * u
        return torch.einsum('bsf,fd->bsd', h, w_down)
    x = A_x
    mod = (_silu(c) @ w_mod).reshape(c.shape[0], 6, 1, -1)
    sa, ca, ga, sb, cb, gb = (mod[:, 0].clone(), mod[:, 1].clone(), mod[:, 2].clone(), mod[:, 3].clone(), mod[:, 4].clone(), mod[:, 5].clone())
    h = _layernorm(x, axis=-1) * (1 + ca) + sa
    x = x + ga * _A_ref(h, A_wqkv, A_wo)
    h = _layernorm(x, axis=-1) * (1 + cb) + sb
    x = x + gb * _B_ref(h, B_w_gate, B_w_up, B_w_down)
    return x
```

```cpp
#include <hip/hip_runtime.h>
#include <hip/hip_bf16.h>
typedef unsigned short u16;
__device__ __forceinline__ float bf2f(u16 v){ return __uint_as_float(((unsigned)v)<<16); }
__device__ __forceinline__ u16 f2bf(float f){ __hip_bfloat16 b = __float2bfloat16(f); return *(u16*)&b; }
#define S 2048
#define D 512
#define F 1376
__global__ void k_mod(const u16* c, const u16* wmod, float* mod){
  int n = blockIdx.x*256+threadIdx.x; float acc=0;
  for(int k=0;k<D;k++){ float cv=bf2f(c[k]); float s=cv/(1.f+__expf(-cv)); s=bf2f(f2bf(s)); acc+=s*bf2f(wmod[k*3072+n]); }
  mod[n]=bf2f(f2bf(acc));
}
__global__ void k_ln(const u16* xb, const float* xf, const float* shift, const float* scale, u16* h){
  int r=blockIdx.x; int t=threadIdx.x; __shared__ float red[256];
  float v0 = xb? bf2f(xb[r*D+t]) : xf[r*D+t]; float v1 = xb? bf2f(xb[r*D+t+256]) : xf[r*D+t+256];
  red[t]=v0+v1; __syncthreads();
  for(int s=128;s>0;s>>=1){ if(t<s) red[t]+=red[t+s]; __syncthreads(); }
  float m=red[0]/D; __syncthreads();
  red[t]=(v0-m)*(v0-m)+(v1-m)*(v1-m); __syncthreads();
  for(int s=128;s>0;s>>=1){ if(t<s) red[t]+=red[t+s]; __syncthreads(); }
  float rs=rsqrtf(red[0]/D+1e-5f);
  h[r*D+t]=f2bf((v0-m)*rs*(1.f+scale[t])+shift[t]);
  h[r*D+t+256]=f2bf((v1-m)*rs*(1.f+scale[t+256])+shift[t+256]);
}
__global__ void k_gemm(const u16* A, const u16* B, float* C, int M,int N,int K){
  int n=blockIdx.x*64+(threadIdx.x&63); int m=blockIdx.y*4+(threadIdx.x>>6);
  if(n>=N) return; float acc=0;
  for(int k=0;k<K;k++) acc+=bf2f(A[m*K+k])*bf2f(B[k*N+n]);
  C[m*N+n]=acc;
}
__global__ void k_attn(const float* qkv, u16* o){
  int s=blockIdx.x; int h=threadIdx.x; if(h>=32) return;
  float lg[8]; float mx=-1e30f;
  #pragma unroll
  for(int w=0;w<8;w++){ int j=s-2*w; float a=-1e9f; if(j>=0){ a=0; for(int d=0;d<16;d++) a+=qkv[s*1536+h*16+d]*qkv[j*1536+512+h*16+d]; a*=0.25f;} lg[w]=a; mx=fmaxf(mx,a);}
  float sum=0;
  #pragma unroll
  for(int w=0;w<8;w++){ lg[w]=__expf(lg[w]-mx); sum+=lg[w]; }
  for(int d=0;d<16;d++){ float a=0;
    #pragma unroll
    for(int w=0;w<8;w++){ int j=s-2*w; if(j>=0) a+=lg[w]*qkv[j*1536+1024+h*16+d]; }
    o[s*D+h*16+d]=f2bf(a/sum); }
}
__global__ void k_res1(const u16* x, const float* y, const float* g, float* x1){ int i=blockIdx.x*256+threadIdx.x; x1[i]=bf2f(x[i])+g[i&511]*y[i]; }
__global__ void k_glu(const float* g,const float* u,u16* h){ int i=blockIdx.x*256+threadIdx.x; h[i]=f2bf(u[i]/(1.f+__expf(-g[i]))); }
__global__ void k_res2(const float* x1, const float* y, const float* g, u16* out){ int i=blockIdx.x*256+threadIdx.x; out[i]=f2bf(x1[i]+g[i&511]*y[i]); }
extern "C" void kernel_launch(void* const* d_in, const int* in_sizes, int n_in, void* d_out, int out_size, void* d_ws, size_t ws_size, hipStream_t stream) {
  const u16* x=(const u16*)d_in[0]; const u16* wqkv=(const u16*)d_in[1]; const u16* wo=(const u16*)d_in[2];
  const u16* wg=(const u16*)d_in[4]; const u16* wu=(const u16*)d_in[5]; const u16* wd=(const u16*)d_in[6];
  const u16* c=(const u16*)d_in[7]; const u16* wmod=(const u16*)d_in[8];
  char* ws=(char*)d_ws; float* mod=(float*)ws; u16* h=(u16*)(ws+(1<<20)); float* qkv=(float*)(ws+(8<<20)); u16* o=(u16*)(ws+(32<<20));
  float* y=(float*)(ws+(40<<20)); float* x1=(float*)(ws+(48<<20)); float* g=(float*)(ws+(64<<20)); float* u=(float*)(ws+(96<<20)); u16* h2=(u16*)(ws+(128<<20));
  k_mod<<<12,256,0,stream>>>(c,wmod,mod);
  k_ln<<<S,256,0,stream>>>(x,nullptr,mod,mod+512,h);
  k_gemm<<<dim3(24,S/4),256,0,stream>>>(h,wqkv,qkv,S,1536,D);
  k_attn<<<S,64,0,stream>>>(qkv,o);
  k_gemm<<<dim3(8,S/4),256,0,stream>>>(o,wo,y,S,D,D);
  k_res1<<<S*D/256,256,0,stream>>>(x,y,mod+1024,x1);
  k_ln<<<S,256,0,stream>>>(nullptr,x1,mod+1536,mod+2048,h);
  k_gemm<<<dim3(22,S/4),256,0,stream>>>(h,wg,g,S,F,D);
  k_gemm<<<dim3(22,S/4),256,0,stream>>>(h,wu,u,S,F,D);
  k_glu<<<S*F/256,256,0,stream>>>(g,u,h2);
  k_gemm<<<dim3(8,S/4),256,0,stream>>>(h2,wd,y,S,D,F);
  k_res2<<<S*D/256,256,0,stream>>>(x1,y,mod+2560,(u16*)d_out);
}
```

```cpp
#include <hip/hip_runtime.h>
#include <hip/hip_bf16.h>
typedef unsigned short u16;
__device__ __forceinline__ float bf2f(u16 v){ return __uint_as_float(((unsigned)v)<<16); }
__device__ __forceinline__ u16 f2bf(float f){ __hip_bfloat16 b = __float2bfloat16(f); return *(u16*)&b; }
#define S 2048
#define D 512
#define F 1376
__global__ void k_mod(const u16* c, const u16* wmod, float* mod){
  int n = blockIdx.x*256+threadIdx.x; float acc=0;
  for(int k=0;k<D;k++){ float cv=bf2f(c[k]); float s=cv/(1.f+__expf(-cv)); s=bf2f(f2bf(s)); acc+=s*bf2f(wmod[k*3072+n]); }
  mod[n]=bf2f(f2bf(acc));
}
__global__ void k_ln(const u16* xb, const float* xf, const float* shift, const float* scale, u16* h){
  int r=blockIdx.x; int t=threadIdx.x; __shared__ float red[256];
  float v0 = xb? bf2f(xb[r*D+t]) : xf[r*D+t]; float v1 = xb? bf2f(xb[r*D+t+256]) : xf[r*D+t+256];
  red[t]=v0+v1; __syncthreads();
  for(int s=128;s>0;s>>=1){ if(t<s) red[t]+=red[t+s]; __syncthreads(); }
  float m=red[0]/D; __syncthreads();
  red[t]=(v0-m)*(v0-m)+(v1-m)*(v1-m); __syncthreads();
  for(int s=128;s>0;s>>=1){ if(t<s) red[t]+=red[t+s]; __syncthreads(); }
  float rs=rsqrtf(red[0]/D+1e-5f);
  h[r*D+t]=f2bf((v0-m)*rs*(1.f+scale[t])+shift[t]);
  h[r*D+t+256]=f2bf((v1-m)*rs*(1.f+scale[t+256])+shift[t+256]);
}
__global__ void k_gemm(const u16* A, const u16* B, float* C, int M,int N,int K){
  int n=blockIdx.x*64+(threadIdx.x&63); int m=blockIdx.y*4+(threadIdx.x>>6);
  if(n>=N) return; float acc=0;
  for(int k=0;k<K;k++) acc+=bf2f(A[m*K+k])*bf2f(B[k*N+n]);
  C[m*N+n]=acc;
}
__global__ void k_attn(const float* qkv, u16* o){
  int s=blockIdx.x; int h=threadIdx.x; if(h>=32) return;
  float lg[8]; float mx=-1e30f;
  #pragma unroll
  for(int w=0;w<8;w++){ int j=s-2*w; float a=-1e9f; if(j>=0){ a=0; for(int d=0;d<16;d++) a+=qkv[s*1536+h*16+d]*qkv[j*1536+512+h*16+d]; a*=0.25f;} lg[w]=a; mx=fmaxf(mx,a);}
  float sum=0;
  #pragma unroll
  for(int w=0;w<8;w++){ lg[w]=__expf(lg[w]-mx); sum+=lg[w]; }
  for(int d=0;d<16;d++){ float a=0;
    #pragma unroll
    for(int w=0;w<8;w++){ int j=s-2*w; if(j>=0) a+=lg[w]*qkv[j*1536+1024+h*16+d]; }
    o[s*D+h*16+d]=f2bf(a/sum); }
}
__global__ void k_res1(const u16* x, const float* y, const float* g, float* x1){ int i=blockIdx.x*256+threadIdx.x; x1[i]=bf2f(x[i])+g[i&511]*y[i]; }
__global__ void k_glu(const float* g,const float* u,u16* h){ int i=blockIdx.x*256+threadIdx.x; h[i]=f2bf(u[i]/(1.f+__expf(-g[i]))); }
__global__ void k_res2(const float* x1, const float* y, const float* g, u16* out){ int i=blockIdx.x*256+threadIdx.x; out[i]=f2bf(x1[i]+g[i&511]*y[i]); }
#define PROBE 3

typedef unsigned long long u64;
typedef __attribute__((ext_vector_type(4))) unsigned u32x4;
__global__ void __launch_bounds__(512) k_probe(const char* x, const char* wqkv, const char* wmod, const char* wg_, unsigned* out){
  int wg = blockIdx.x, t = threadIdx.x;
  u64 t0 = __builtin_amdgcn_s_memrealtime();
  unsigned acc = 0;
  if(acc==0x12345678) out[4096]=1;
  __syncthreads();
  u64 t1 = __builtin_amdgcn_s_memrealtime();
  if(t==0){ out[wg*2]=(unsigned)t0; out[wg*2+1]=(unsigned)t1; }
}
__global__ void k_spin(const unsigned* in){
  unsigned mn=0xffffffffu, mx=0; unsigned b = in[0];
  for(int i=0;i<256;i++){ unsigned a0=in[2*i]-b+100000u, a1=in[2*i+1]-b+100000u; mn=min(mn,a0); mx=max(mx,a1);}
  u64 d = (u64)(mx-mn); if(d>3000) d=3000;
  u64 t0 = __builtin_amdgcn_s_memrealtime();
  while(__builtin_amdgcn_s_memrealtime() - t0 < d*1000) __builtin_amdgcn_s_sleep(8);
}

extern "C" void kernel_launch(void* const* d_in, const int* in_sizes, int n_in, void* d_out, int out_size, void* d_ws, size_t ws_size, hipStream_t stream) {
  const u16* x=(const u16*)d_in[0]; const u16* wqkv=(const u16*)d_in[1]; const u16* wo=(const u16*)d_in[2];
  const u16* wg=(const u16*)d_in[4]; const u16* wu=(const u16*)d_in[5]; const u16* wd=(const u16*)d_in[6];
  const u16* c=(const u16*)d_in[7]; const u16* wmod=(const u16*)d_in[8];
  char* ws=(char*)d_ws; float* mod=(float*)ws; u16* h=(u16*)(ws+(1<<20)); float* qkv=(float*)(ws+(8<<20)); u16* o=(u16*)(ws+(32<<20));
  float* y=(float*)(ws+(40<<20)); float* x1=(float*)(ws+(48<<20)); float* g=(float*)(ws+(64<<20)); float* u=(float*)(ws+(96<<20)); u16* h2=(u16*)(ws+(128<<20));
  k_probe<<<256,512,0,stream>>>((const char*)x,(const char*)wqkv,(const char*)wmod,(const char*)wg,(unsigned*)(ws+(200<<20)));
  k_spin<<<1,1,0,stream>>>((unsigned*)(ws+(200<<20)));
  k_mod<<<12,256,0,stream>>>(c,wmod,mod);
  k_ln<<<S,256,0,stream>>>(x,nullptr,mod,mod+512,h);
  k_gemm<<<dim3(24,S/4),256,0,stream>>>(h,wqkv,qkv,S,1536,D);
  k_attn<<<S,64,0,stream>>>(qkv,o);
  k_gemm<<<dim3(8,S/4),256,0,stream>>>(o,wo,y,S,D,D);
  k_res1<<<S*D/256,256,0,stream>>>(x,y,mod+1024,x1);
  k_ln<<<S,256,0,stream>>>(nullptr,x1,mod+1536,mod+2048,h);
  k_gemm<<<dim3(22,S/4),256,0,stream>>>(h,wg,g,S,F,D);
  k_gemm<<<dim3(22,S/4),256,0,stream>>>(h,wu,u,S,F,D);
  k_glu<<<S*F/256,256,0,stream>>>(g,u,h2);
  k_gemm<<<dim3(8,S/4),256,0,stream>>>(h2,wd,y,S,D,F);
  k_res2<<<S*D/256,256,0,stream>>>(x1,y,mod+2560,(u16*)d_out);
}
```

```cpp
#include <hip/hip_runtime.h>

#define DI __device__ __forceinline__
#define LAS __attribute__((address_space(3)))
typedef unsigned u32;
typedef unsigned long long u64;
typedef u32 u32x2 __attribute__((ext_vector_type(2)));
typedef u32 u32x4 __attribute__((ext_vector_type(4)));
typedef float f32x2 __attribute__((ext_vector_type(2)));
typedef float f32x4 __attribute__((ext_vector_type(4)));
typedef short s16x4 __attribute__((ext_vector_type(4)));
typedef __bf16 bf16x2 __attribute__((ext_vector_type(2)));
typedef __bf16 bf16x8 __attribute__((ext_vector_type(8)));
typedef __amdgpu_buffer_rsrc_t srd_t;

constexpr u32 WS_EPOCH = 0, WS_HELLO = 4096, WS_MODP = 8192, WS_FLAG = 65536, WS_STAT = 0x100000, WS_O = 0x200000,
              WS_X1 = 0x400000, WS_H2 = 0x600000, WS_STAMP = 0xC00000, WS_END = 0xD00000;
constexpr u32 H2_RB = 172 * 256;
constexpr u32 OOB = 0x80000000u;
constexpr int SC1 = 16;
constexpr u32 L_H = 0, L_S = 81920, L_DUMP = 147456, L_CNT = 157696, L_SB = 157952, L_RS = 162048, L_G3 = 163072;
constexpr u32 L_UV = L_SB + 2048, L_X0 = L_S + 32768, L_X1 = L_H, L_GD = L_H + 32768;
enum { cW = 0, cWc, cM1, cM2, cM3, cG, cR, cXs, cS = 8, cPo = 10, cPx = 12, cTag = 14, cQ = 16, cQf = 20, cWo = 24, cDf = 28, cF = 32,
       cH = 36, cA = 40, cB = 44, cX = 48, cPh = 52 };
#define LCAP (1u << 16)
#define GCAP (1u << 12)

#define STAMP(k) do {} while (0)

DI u32 rfl(u32 x) { return __builtin_amdgcn_readfirstlane(x); }
DI u32 opq(u32 x) { asm volatile("" : "+v"(x)); return x; }
DI float bflo(u32 v) { return __builtin_bit_cast(float, v << 16); }
DI float bfhi(u32 v) { return __builtin_bit_cast(float, v & 0xffff0000u); }
DI u32 cvtpk(float lo, float hi) { f32x2 v = {lo, hi}; return __builtin_bit_cast(u32, __builtin_convertvector(v, bf16x2)); }
DI float rbf(float x) { return bflo(cvtpk(x, x)); }
DI f32x4 mfma32(u32x4 a, u32x4 b, f32x4 c) {
  return __builtin_amdgcn_mfma_f32_16x16x32_bf16(__builtin_bit_cast(bf16x8, a), __builtin_bit_cast(bf16x8, b), c, 0, 0, 0);
}
DI f32x4 mfma16(u32x2 a, u32x2 b, f32x4 c) {
  return __builtin_amdgcn_mfma_f32_16x16x16bf16_1k(__builtin_bit_cast(s16x4, a), __builtin_bit_cast(s16x4, b), c, 0, 0, 0);
}
template <int C> DI float dpp(float x) {
  return __builtin_bit_cast(float, __builtin_amdgcn_update_dpp(0, __builtin_bit_cast(int, x), C, 0xf, 0xf, true));
}
DI float fu(u32 x) { return __builtin_bit_cast(float, x); }
DI u32 uf(float x) { return __builtin_bit_cast(u32, x); }
DI float sum16(float x) { auto r = __builtin_amdgcn_permlane16_swap(uf(x), uf(x), false, false); return fu(r[0]) + fu(r[1]); }
DI float sum32(float x) { auto r = __builtin_amdgcn_permlane32_swap(uf(x), uf(x), false, false); return fu(r[0]) + fu(r[1]); }
DI float max16(float x) { auto r = __builtin_amdgcn_permlane16_swap(uf(x), uf(x), false, false); return fmaxf(fu(r[0]), fu(r[1])); }
DI float max32(float x) { auto r = __builtin_amdgcn_permlane32_swap(uf(x), uf(x), false, false); return fmaxf(fu(r[0]), fu(r[1])); }

template <class T> DI T lds_ld(u32 a) { return *(LAS T*)a; }
template <class T> DI void lds_st(u32 a, T v) { *(LAS T*)a = v; }
DI u32x4 tr_frag(u32 a, u32 hstep) {
  s16x4 lo = __builtin_amdgcn_ds_read_tr16_b64_v4i16((LAS s16x4*)a);
  s16x4 hi = __builtin_amdgcn_ds_read_tr16_b64_v4i16((LAS s16x4*)(a + hstep));
  u32x2 l = __builtin_bit_cast(u32x2, lo), h = __builtin_bit_cast(u32x2, hi);
  return u32x4{l.x, l.y, h.x, h.y};
}
DI void dma(srd_t r, u32 lds, u32 voff, u32 soff) {
  asm volatile("s_mov_b32 m0, %0\n\ts_nop 0\n\tbuffer_load_dwordx4 %1, %2, %3 offen lds" ::"s"(lds), "v"(voff), "s"(r), "s"(soff) : "memory");
}
DI void dma_sc1(srd_t r, u32 lds, u32 voff, u32 soff) {
  asm volatile("s_mov_b32 m0, %0\n\ts_nop 0\n\tbuffer_load_dwordx4 %1, %2, %3 offen sc1 lds" ::"s"(lds), "v"(voff), "s"(r), "s"(soff) : "memory");
}
template <int N> DI void vmwait() { asm volatile("s_waitcnt vmcnt(%0)" ::"i"(N) : "memory"); }
DI void lgwait() { asm volatile("s_waitcnt lgkmcnt(0)" ::: "memory"); }
DI void arrive(u32 a) {
  asm volatile("s_mov_b64 exec, 1\n\tds_add_u32 %0, %1\n\ts_mov_b64 exec, -1" ::"v"(a), "v"(1u) : "memory");
}
DI u32 arrive_rtn(u32 a) {
  u32 v, s;
  asm volatile("s_mov_b64 exec, 1\n\tds_add_rtn_u32 %0, %2, %3\n\ts_mov_b64 exec, -1\n\ts_waitcnt lgkmcnt(0)\n\tv_readfirstlane_b32 %1, %0\n\ts_nop 0"
               : "=&v"(v), "=s"(s) : "v"(a), "v"(1u) : "memory");
  return s;
}
DI void lwait(u32 a, u32 target) {
  u32 v, s, n = LCAP;
  asm volatile("1:\n\tds_read_b32 %0, %3\n\ts_waitcnt lgkmcnt(0)\n\tv_readfirstlane_b32 %1, %0\n\ts_nop 0\n\ts_cmp_ge_u32 %1, %4\n\ts_cbranch_scc1 2f\n\t"
               "s_sub_u32 %2, %2, 1\n\ts_cbranch_scc0 1b\n2:"
               : "=&v"(v), "=&s"(s), "+s"(n) : "v"(a), "s"(target) : "memory", "scc");
}
#define CNT(i) (L_CNT + 4u * (i))
DI void gpoll(srd_t r, u32 voff, u64 tag) {
  u64 v; u32 n = GCAP;
  asm volatile("1:\n\tbuffer_load_dwordx2 %0, %2, %3, 0 offen sc1\n\ts_waitcnt vmcnt(0)\n\tv_cmp_ne_u64 vcc, %4, %0\n\ts_nop 1\n\ts_cmp_eq_u64 vcc, 0\n\t"
               "s_cbranch_scc1 2f\n\ts_sub_u32 %1, %1, 1\n\ts_cbranch_scc0 1b\n2:"
               : "=&v"(v), "+s"(n) : "v"(voff), "s"(r), "s"(tag) : "memory", "scc", "vcc");
}
#define SB() __builtin_amdgcn_sched_barrier(0)
DI u32 lane_id() { u32 l; asm volatile("v_mbcnt_lo_u32_b32 %0, -1, 0\n\tv_mbcnt_hi_u32_b32 %0, -1, %0" : "=v"(l)); return l; }
DI void st16_sc1(u32x4 v, srd_t r, u32 voff, u32 soff) {
  asm volatile("buffer_store_dwordx4 %0, %1, %2, %3 offen sc1\n\ts_nop 1" ::"v"(v), "v"(voff), "s"(r), "s"(soff) : "memory");
}
DI void st16(u32x4 v, srd_t r, u32 voff, u32 soff) {
  asm volatile("buffer_store_dwordx4 %0, %1, %2, %3 offen\n\ts_nop 1" ::"v"(v), "v"(voff), "s"(r), "s"(soff) : "memory");
}
DI void gflag(srd_t r, u32 voff, u64 tag) {
  __builtin_amdgcn_raw_buffer_store_b64(u32x2{(u32)tag, (u32)(tag >> 32)}, r, voff, 0, SC1);
}
DI f32x4 gather_mod(srd_t ws, u32 col, u64 tag) {
  const u32 t0 = tag & 1023, t1 = (tag >> 10) & 1023, t2 = (tag >> 20) & 1023, t3 = (tag >> 30) & 1023;
  u32x4 p[4];
  for (u32 n = 0; n < GCAP; ++n) {
    asm volatile("" ::: "memory");
    u32 bad = 0;
#pragma unroll
    for (int k = 0; k < 4; ++k) p[k] = __builtin_amdgcn_raw_buffer_load_b128(ws, col * 4, WS_MODP + k * 12288, SC1);
#pragma unroll
    for (int k = 0; k < 4; ++k) bad |= (p[k].x ^ t0) | (p[k].y ^ t1) | (p[k].z ^ t2) | (p[k].w ^ t3);
    if (__builtin_amdgcn_ballot_w64((bad & 1023) != 0) == 0) break;
  }
  f32x4 r;
#pragma unroll
  for (int j = 0; j < 4; ++j) r[j] = rbf((fu(p[0][j] & ~1023u) + fu(p[1][j] & ~1023u)) + (fu(p[2][j] & ~1023u) + fu(p[3][j] & ~1023u)));
  return r;
}

struct Params {
  const void *x, *wqkv, *wo, *wg, *wu, *wd, *c, *wmod;
  void *out, *ws;
};

__global__ void __launch_bounds__(512, 2) adaln_block(Params P) {
  __shared__ __attribute__((aligned(16))) char smem[163840];
  const u32 tid = threadIdx.x, l = tid & 63, wave = rfl(tid >> 6), w = wave & 3;
  const u32 wg = blockIdx.x, xcd = wg & 7, r = wg >> 3, tq = r >> 3, cg = r & 7, fg = r & 15;
  const u32 T1 = xcd * 256 + tq * 64, T3 = xcd * 256 + (r >> 4) * 128;
  const u32 q = l >> 4, tok = l & 15;
  asm volatile("" ::"v"((u32)(size_t)(LAS char*)smem) : "memory");
  STAMP(0);
  const srd_t ws = __builtin_amdgcn_make_buffer_rsrc(P.ws, 0, WS_END, 0x00020000);
  const srd_t rx = __builtin_amdgcn_make_buffer_rsrc((void*)P.x, 0, 2048 * 1024, 0x00020000);
  const u32 jj = (l >> 2) & 3, fsw = (jj >> 1) | ((q & 1) << 1);
  const u32 trb = (8 * q + jj) * 128 + (l & 3) * 8;
  const u32 drow = l >> 3, dch0 = (l & 7) ^ (((drow >> 1) & 1) << 1);

  if (wave < 4) {
    if (wave == 0) lds_st<u32>(L_CNT + 4 * l, 0u);
    lgwait();
    __builtin_amdgcn_s_barrier();
    asm volatile("" ::: "memory");
    u32x4 W[48];
    f32x4 x1r[2];
    u64 tag;
    {
      const srd_t rw = __builtin_amdgcn_make_buffer_rsrc((void*)P.wqkv, 0, 512 * 3072, 0x00020000);
      const u32 v0 = (16 * w + drow) * 3072 + cg * 128 + dch0 * 16, v1 = (v0 ^ 64) + 8 * 3072;
      auto issue = [&](int c) {
        const u32 so = ((c + xcd) & 7) * (64 * 3072), d = L_S + (c % 3) * 24576 + w * 2048;
#pragma unroll
        for (int s = 0; s < 3; ++s) { dma(rw, d + s * 8192, v0, so + s * 1024); dma(rw, d + s * 8192 + 1024, v1, so + s * 1024); }
      };
      const u32 tb = L_S + trb + ((w ^ fsw) << 5);
      auto rd = [&](int c) {
#pragma unroll
        for (int kk = 0; kk < 2; ++kk)
#pragma unroll
          for (int s = 0; s < 3; ++s) W[(2 * c + kk) * 3 + s] = tr_frag(tb + (c % 3) * 24576 + s * 8192 + kk * 4096, 512);
      };
      issue(0); issue(1); issue(2);
      vmwait<12>(); arrive(CNT(cW)); lwait(CNT(cW), 4); rd(0);
      STAMP(1);
#pragma unroll
      for (int c = 1; c < 6; ++c) { vmwait<6>(); arrive(CNT(cW)); lwait(CNT(cW), 4 * c + 4); issue(c + 2); rd(c); }
      arrive(CNT(cWc));
      STAMP(2);
      const u32 he = q * 256 + ((tok ^ q) << 4), ho = q * 256 + ((tok ^ (4 + q)) << 4) + 1024;
      auto hf = [&](u32 base, int ks) { return lds_ld<u32x4>(base + ((ks & 1) ? ho : he) + (ks >> 1) * 2048); };
      const u32 dump = L_DUMP + w * 2560 + l * 8;
      const f32x4 z4 = {0.f, 0.f, 0.f, 0.f};
      lwait(CNT(cH), 4);
      STAMP(3);
      {
        f32x4 aq = z4, ak = z4, av = z4, hk = z4, hv = z4;
        auto step0 = [&](int ks) {
          u32x4 a = hf(L_H + 16384, ks), b = hf(L_H, ks);
          aq = mfma32(W[ks * 3], a, aq); ak = mfma32(W[ks * 3 + 1], a, ak); av = mfma32(a, W[ks * 3 + 2], av);
          hk = mfma32(W[ks * 3 + 1], b, hk); hv = mfma32(b, W[ks * 3 + 2], hv);
        };
#pragma unroll
        for (int ks = 0; ks < 6; ++ks) step0(ks);
        vmwait<6>(); arrive(CNT(cW)); lwait(CNT(cW), 28); rd(6); arrive(CNT(cWc));
#pragma unroll
        for (int ks = 6; ks < 12; ++ks) step0(ks);
        vmwait<0>(); arrive(CNT(cW)); lwait(CNT(cW), 32); rd(7); arrive(CNT(cWc));
        STAMP(4);
#pragma unroll
        for (int ks = 12; ks < 16; ++ks) step0(ks);
        lds_st<u32x2>(dump, u32x2{cvtpk(aq[0], aq[1]), cvtpk(aq[2], aq[3])});
        lds_st<u32x2>(dump + 512, u32x2{cvtpk(ak[0], ak[1]), cvtpk(ak[2], ak[3])});
        lds_st<u32x2>(dump + 1024, u32x2{cvtpk(av[0], av[1]), cvtpk(av[2], av[3])});
        lds_st<u32x2>(dump + 1536, u32x2{cvtpk(hk[0], hk[1]), cvtpk(hk[2], hk[3])});
        lds_st<u32x2>(dump + 2048, u32x2{cvtpk(hv[0], hv[1]), cvtpk(hv[2], hv[3])});
        arrive(CNT(cQ + w)); arrive(CNT(cA));
      }
      STAMP(5);
#pragma unroll 1
      for (u32 g = 1; g < 4; ++g) {
        lwait(CNT(cH + g), 4);
        f32x4 aq = z4, ak = z4, av = z4;
        const u32 hb = L_H + 16384 * (g + 1);
#pragma unroll
        for (int ks = 0; ks < 16; ++ks) {
          u32x4 a = hf(hb, ks);
          aq = mfma32(W[ks * 3], a, aq); ak = mfma32(W[ks * 3 + 1], a, ak); av = mfma32(a, W[ks * 3 + 2], av);
        }
        lwait(CNT(cQf + w), g);
        lds_st<u32x2>(dump, u32x2{cvtpk(aq[0], aq[1]), cvtpk(aq[2], aq[3])});
        lds_st<u32x2>(dump + 512, u32x2{cvtpk(ak[0], ak[1]), cvtpk(ak[2], ak[3])});
        lds_st<u32x2>(dump + 1024, u32x2{cvtpk(av[0], av[1]), cvtpk(av[2], av[3])});
        arrive(CNT(cQ + w)); arrive(CNT(cA + g));
      }
      STAMP(6);
      u32x2 tg = lds_ld<u32x2>(CNT(cTag));
      tag = (u64)rfl(tg.x) | ((u64)rfl(tg.y) << 32);
    }
    const u32 c8 = l & 7, erow = 8 * w + (l >> 3);
    const u32 rwr = tok * 256, rsw = tok & 7;
    const u32 rtk = 8 * (w & 1) + (l >> 3);
    const u32 rrd0 = rtk * 256 + (((2 * c8) ^ (rtk & 7)) << 4), rrd1 = rrd0 ^ 16;
    u32 rsync = 0;
    {
      u32x4 xres[2];
#pragma unroll
      for (int j = 0; j < 2; ++j) xres[j] = __builtin_amdgcn_raw_buffer_load_b128(rx, erow * 1024 + c8 * 16, (T1 + 32 * j) * 1024 + cg * 128, 0);
      lwait(CNT(cWo + w), 1);
#pragma unroll
      for (int i = 0; i < 4; ++i)
#pragma unroll
        for (int cb = 0; cb < 4; ++cb) W[i * 4 + cb] = tr_frag(L_S + i * 16384 + w * 4096 + trb + ((cb ^ fsw) << 5), 512);
      arrive(CNT(cR)); rsync += 4;
      lwait(CNT(cM3), 4);
      lwait(CNT(cQf + w), 4);
      STAMP(7);
#pragma unroll
      for (int j = 0; j < 2; ++j) {
        gpoll(ws, WS_FLAG + wg * 2048 + w * 128 + j * 16 + (l & 1) * 8, tag);
        if (j == 0) STAMP(8); else STAMP(12);
        u32x4 a[2][4];
#pragma unroll
        for (int i = 0; i < 4; ++i)
#pragma unroll
          for (int rb = 0; rb < 2; ++rb)
            a[rb][i] = __builtin_amdgcn_raw_buffer_load_b128(ws, l * 16, WS_O + ((T1 >> 4) + 2 * j + rb) * 16384 + (4 * w + ((i + xcd) & 3)) * 1024, SC1);
        f32x4 acc[2][4] = {};
#pragma unroll
        for (int i = 0; i < 4; ++i)
#pragma unroll
          for (int rb = 0; rb < 2; ++rb)
#pragma unroll
            for (int cb = 0; cb < 4; ++cb) acc[rb][cb] = mfma32(W[i * 4 + cb], a[rb][i], acc[rb][cb]);
        lwait(CNT(cR), rsync);
#pragma unroll
        for (int rb = 0; rb < 2; ++rb)
#pragma unroll
          for (int cb = 0; cb < 4; ++cb) lds_st<f32x4>(L_S + (rb * 4 + w) * 4096 + rwr + (((4 * cb + q) ^ rsw) << 4), acc[rb][cb]);
        arrive(CNT(cR)); rsync += 4;
        if (j == 0) STAMP(9); else STAMP(13);
        lwait(CNT(cR), rsync);
        f32x4 y0, y1;
        {
          const u32 b = L_S + (w >> 1) * 16384;
          y0 = (lds_ld<f32x4>(b + rrd0) + lds_ld<f32x4>(b + 4096 + rrd0)) + (lds_ld<f32x4>(b + 8192 + rrd0) + lds_ld<f32x4>(b + 12288 + rrd0));
          y1 = (lds_ld<f32x4>(b + rrd1) + lds_ld<f32x4>(b + 4096 + rrd1)) + (lds_ld<f32x4>(b + 8192 + rrd1) + lds_ld<f32x4>(b + 12288 + rrd1));
        }
        const f32x4 ga0 = lds_ld<f32x4>(L_G3 + c8 * 32), ga1 = lds_ld<f32x4>(L_G3 + c8 * 32 + 16);
        const f32x4 s0 = lds_ld<f32x4>(L_G3 + 256 + c8 * 32), s1 = lds_ld<f32x4>(L_G3 + 256 + c8 * 32 + 16);
        arrive(CNT(cR)); rsync += 4;
        const f32x4 u0 = f32x4{bflo(xres[j].x), bfhi(xres[j].x), bflo(xres[j].y), bfhi(xres[j].y)} + ga0 * y0;
        const f32x4 u1 = f32x4{bflo(xres[j].z), bfhi(xres[j].z), bflo(xres[j].w), bfhi(xres[j].w)} + ga1 * y1;
        if (j == 0) { lds_st<f32x4>(L_DUMP + w * 2560 + l * 16, u0); lds_st<f32x4>(L_DUMP + w * 2560 + 1024 + l * 16, u1); }
        else { x1r[0] = u0; x1r[1] = u1; }
        const f32x4 m0 = u0 * s0, m1 = u1 * s1;
        st16_sc1(u32x4{cvtpk(m0[0], m0[1]), cvtpk(m0[2], m0[3]), cvtpk(m1[0], m1[1]), cvtpk(m1[2], m1[3])}, ws,
                                               erow * 1024 + c8 * 16, WS_X1 + (T1 + 32 * j) * 1024 + cg * 128);
        const f32x4 sv = u0 + u1, qv = u0 * u0 + u1 * u1;
        float sm = (sv[0] + sv[1]) + (sv[2] + sv[3]), sq = (qv[0] + qv[1]) + (qv[2] + qv[3]);
        sm += dpp<0xB1>(sm); sq += dpp<0xB1>(sq);
        sm += dpp<0x4E>(sm); sq += dpp<0x4E>(sq);
        sm += dpp<0x141>(sm); sq += dpp<0x141>(sq);
        __builtin_amdgcn_raw_buffer_store_b64(u32x2{uf(sm), uf(sq)}, ws, c8 == 0 ? rtk * 8 : OOB,
                                              WS_STAT + (((T1 >> 4) + 2 * j + (w >> 1)) * 8 + cg) * 128, SC1);
        if (j == 0) STAMP(10); else STAMP(14);
        vmwait<0>();
        if (arrive_rtn(CNT(cPx + j)) == 3)
          gflag(ws, l < 16 ? WS_FLAG + (((r & 16) + l) * 8 + xcd) * 2048 + (4 + j) * 128 + fg * 8 : OOB, tag);
        if (j == 0) STAMP(11); else STAMP(15);
      }
    }
    {
      const srd_t rd = __builtin_amdgcn_make_buffer_rsrc((void*)P.wd, 0, 1376 * 1024, 0x00020000);
      const u32 v0 = drow * 1024 + cg * 128 + dch0 * 16;
      auto wd_issue = [&](int rnd) {
#pragma unroll
        for (int i = 2 * rnd; i < 2 * rnd + 2 && i < 11; ++i) {
          u32 li = i + xcd; li = li >= 11 ? li - 11 : li;
          const u32 ks = 11 * w + li, ve = ks > 42 ? OOB : v0, vo = ve ^ 64, so = ks > 42 ? 0 : ks * 32768;
          const u32 d = L_S + w * 8192 + (i & 1) * 4096;
          dma(rd, d, ve, so); dma(rd, d + 1024, vo, so + 8192); dma(rd, d + 2048, ve, so + 16384); dma(rd, d + 3072, vo, so + 24576);
        }
      };
      const u32 wdb = L_S + w * 8192 + trb + (fsw << 5);
      auto wd_read = [&](int rnd) {
        const u32 b0 = opq(wdb);
#pragma unroll
        for (int cb = 0; cb < 4; ++cb) {
          const u32 b = opq(b0 ^ (cb << 5));
#pragma unroll
          for (int i = 2 * rnd; i < 2 * rnd + 2 && i < 11; ++i) W[i * 4 + cb] = tr_frag(b + (i & 1) * 4096, 512);
        }
        lgwait();
      };
      auto stats = [&](int j) {
        u32x4 p = __builtin_amdgcn_raw_buffer_load_b128(ws, l * 16, WS_STAT + ((T3 >> 4) + 4 * (w >> 1) + 2 * j + (w & 1)) * 1024, SC1);
        float v[4];
#pragma unroll
        for (int k = 0; k < 4; ++k) { float t = fu(p[k]); t += dpp<0x128>(t); v[k] = sum32(sum16(t)); }
        const float m0 = v[0] * (1.f / 512), m1 = v[2] * (1.f / 512);
        const float r0 = __builtin_amdgcn_rsqf(fmaxf(v[1] * (1.f / 512) - m0 * m0, 0.f) + 1e-5f);
        const float r1 = __builtin_amdgcn_rsqf(fmaxf(v[3] * (1.f / 512) - m1 * m1, 0.f) + 1e-5f);
        lds_st<f32x4>(L_RS + ((2 * j + (w & 1)) * 2 + (w >> 1)) * 128 + (l & 7) * 16, f32x4{r0, m0, r1, m1});
        arrive(CNT(cS));
      };
      const u32 xv = (((l ^ (8 * (w & 1))) + 8 * xcd) & 63) * 16;
      auto x1_issue = [&](int s) {
        const u32 d = ((s & 1) ? L_X1 : L_X0) + w * 8192, so = WS_X1 + (T3 + 64 * (w >> 1) + 16 * s + 8 * (w & 1)) * 1024;
#pragma unroll
        for (int i = 0; i < 8; ++i) dma_sc1(ws, d + i * 1024, xv ^ (i << 4), so + i * 1024);
      };
      auto glu = [&](f32x4 g, f32x4 u, u32 uvb, float rs, float mu) {
        const f32x4 ug = lds_ld<f32x4>(uvb), uu = lds_ld<f32x4>(uvb + 32), vg = lds_ld<f32x4>(uvb + 64), vu = lds_ld<f32x4>(uvb + 96);
        const float rn = rs * -1.4426950408889634f;
        float h[4];
#pragma unroll
        for (int k = 0; k < 4; ++k) {
          const float e = __builtin_amdgcn_exp2f(fmaf(rn, fmaf(-mu, ug[k], g[k]), vg[k]));
          h[k] = fmaf(rs, fmaf(-mu, uu[k], u[k]), vu[k]) * __builtin_amdgcn_rcpf(1.f + e);
        }
        return u32x2{cvtpk(h[0], h[1]), cvtpk(h[2], h[3])};
      };
      auto epilogue = [&](int s) {
        const u32 l2 = opq(l), ti = l2 >> 5, bq = (l2 >> 4) & 1, tok = l2 & 15;
        const u32 db = L_GD + (s & 1) * 24576 + w * 6144 + ti * 3072 + tok * 16;
        const f32x2 st = lds_ld<f32x2>(L_RS + (s * 2 + ti) * 128 + tok * 8);
        const u32 d01 = opq(db + bq * 1024), d2 = opq(db + 2048 + bq * 256), uv01 = opq(L_UV + w * 384 + bq * 128), uv2 = opq(L_UV + w * 384 + 256 + bq * 16);
        const u32x2 h0 = glu(lds_ld<f32x4>(d01), lds_ld<f32x4>(d01 + 512), uv01, st[0], st[1]);
        __builtin_amdgcn_sched_barrier(0);
        const u32x2 h1 = glu(lds_ld<f32x4>(d01 + 256), lds_ld<f32x4>(d01 + 768), uv01 + 16, st[0], st[1]);
        __builtin_amdgcn_sched_barrier(0);
        const u32x2 h2 = glu(lds_ld<f32x4>(d2), lds_ld<f32x4>(d2 + 512), uv2, st[0], st[1]);
        arrive(CNT(cDf + w));
        const u32 so = WS_H2 + ((T3 >> 4) + s) * H2_RB, pc = 12 * fg + 3 * w, vb = ti * (4 * H2_RB) + tok * 16;
        st16_sc1(u32x4{h0.x, h0.y, h1.x, h1.y}, ws, pc + bq < 172 ? vb + (pc + bq) * 256 : OOB, so);
        auto ex = __builtin_amdgcn_permlane16_swap(h2.x, h2.x, false, false);
        auto ey = __builtin_amdgcn_permlane16_swap(h2.y, h2.y, false, false);
        st16_sc1(u32x4{ex[0], ey[0], ex[1], ey[1]}, ws, (bq == 0 && pc + 2 < 172) ? vb + (pc + 2) * 256 : OOB, so);
      };
      auto publish_h2 = [&](int s) {
        if (arrive_rtn(CNT(cPh + s)) == 3) {
          const u32 w1 = (12 * fg) / 44, w2 = (12 * fg + 11) / 44, wc = l < 16 ? w1 : w2;
          const u32 first = wc == 0 ? 0 : 4 * wc - 1;
          const bool on = l < 16 || (l < 32 && w2 != w1 && w2 < 4);
          gflag(ws, on ? WS_FLAG + (((r & 16) + tok) * 8 + xcd) * 2048 + (8 + 2 * wc + (s >> 1)) * 128 + (s & 1) * 64 + (fg - first) * 8 : OOB, tag);
        }
      };
      lwait(CNT(cR), rsync);
      if (fg != 15) {
        lwait(CNT(cXs), 1);
        STAMP(16);
        stats(0);
        wd_issue(0);
        if (w == 0) { gpoll(ws, WS_FLAG + wg * 2048 + 5 * 128 + tok * 8, tag); arrive(CNT(cXs)); }
        vmwait<0>(); wd_read(0); wd_issue(1);
        lwait(CNT(cXs), 2);
        stats(1);
        vmwait<0>(); wd_read(1); wd_issue(2);
        lwait(CNT(cS), 8);
        STAMP(17);
        lwait(CNT(cB), 4); STAMP(18); x1_issue(2); epilogue(0);
        vmwait<10>(); wd_read(2); wd_issue(3);
        vmwait<8>(); arrive(CNT(cX + 2)); publish_h2(0);
        STAMP(19);
        vmwait<0>(); wd_read(3); wd_issue(4);
        lwait(CNT(cB + 1), 4); STAMP(20); x1_issue(3); epilogue(1);
        vmwait<10>(); wd_read(4); wd_issue(5);
        vmwait<4>(); arrive(CNT(cX + 3)); publish_h2(1);
        STAMP(21);
        vmwait<0>(); wd_read(5);
        lwait(CNT(cB + 2), 4); STAMP(22); epilogue(2);
        vmwait<0>(); publish_h2(2);
        STAMP(23);
        lwait(CNT(cB + 3), 4); STAMP(24); epilogue(3);
        STAMP(25);
      } else {
#pragma unroll
        for (int rnd = 0; rnd < 6; ++rnd) { wd_issue(rnd); vmwait<0>(); wd_read(rnd); }
      }
      const srd_t ro = __builtin_amdgcn_make_buffer_rsrc(P.out, 0, 2048 * 1024, 0x00020000);
      u32x4 a[8];
      auto h2_ld = [&](int n) {
        const int g = n / 11, i = n % 11;
        u32 li = i + xcd; li = li >= 11 ? li - 11 : li;
        u32 ks = 11 * w + li; ks = ks > 42 ? 42 : ks;
        a[n & 7] = __builtin_amdgcn_raw_buffer_load_b128(ws, l * 16, WS_H2 + ((T1 >> 4) + g) * H2_RB + ks * 1024, SC1);
      };
      lwait(CNT(cF + w), 1);
#pragma unroll
      for (int n = 0; n < 8; ++n) h2_ld(n);
      if (fg != 15) { vmwait<8>(); publish_h2(3); }
      STAMP(26);
#pragma unroll
      for (int g = 0; g < 4; ++g) {
        f32x4 acc[4] = {};
        if (g < 2) lwait(CNT(cF + w), g + 2);
#pragma unroll
        for (int i = 0; i < 11; ++i) {
          const int n = 11 * g + i;
#pragma unroll
          for (int cb = 0; cb < 4; ++cb) acc[cb] = mfma32(W[i * 4 + cb], a[n & 7], acc[cb]);
          if (n + 8 < 33 || (g == 3 && n + 8 < 44)) h2_ld(n + 8);
          __builtin_amdgcn_sched_barrier(0);
        }
        const u32 l2 = opq(l);
        {
          const u32 wa = opq(L_X0 + (g & 1) * 16384 + w * 4096 + (l2 & 15) * 256 + (((l2 >> 4) ^ (l2 & 7)) << 4)), wb = wa ^ 64;
          lds_st<f32x4>(wa, acc[0]); lds_st<f32x4>(wb, acc[1]); lds_st<f32x4>(wa + 128, acc[2]); lds_st<f32x4>(wb + 128, acc[3]);
        }
        arrive(CNT(cR)); rsync += 4;
        if (g == 3) STAMP(30);
        lwait(CNT(cR), rsync);
        if ((w >> 1) == (g & 1)) {
          const u32 c8 = l2 & 7, rtk = 8 * (w & 1) + (l2 >> 3), erow = 8 * w + (l2 >> 3), rb = 0;
          const u32 rrd0 = opq(L_X0 + (g & 1) * 16384 + rtk * 256 + (((2 * c8) ^ (rtk & 7)) << 4)), rrd1 = rrd0 ^ 16;
          const u32 gba = opq(L_G3 + 512 + c8 * 32), xa = opq(L_DUMP + w * 2560 + l2 * 16);
          const f32x4 y0 = (lds_ld<f32x4>(rb + rrd0) + lds_ld<f32x4>(rb + 4096 + rrd0)) + (lds_ld<f32x4>(rb + 8192 + rrd0) + lds_ld<f32x4>(rb + 12288 + rrd0));
          const f32x4 o0 = (g < 2 ? lds_ld<f32x4>(xa) : x1r[0]) + lds_ld<f32x4>(gba) * y0;
          __builtin_amdgcn_sched_barrier(0);
          const f32x4 y1 = (lds_ld<f32x4>(rb + rrd1) + lds_ld<f32x4>(rb + 4096 + rrd1)) + (lds_ld<f32x4>(rb + 8192 + rrd1) + lds_ld<f32x4>(rb + 12288 + rrd1));
          const f32x4 o1 = (g < 2 ? lds_ld<f32x4>(xa + 1024) : x1r[1]) + lds_ld<f32x4>(gba + 16) * y1;
          st16(u32x4{cvtpk(o0[0], o0[1]), cvtpk(o0[2], o0[3]), cvtpk(o1[0], o1[1]), cvtpk(o1[2], o1[3])}, ro,
                                                 erow * 1024 + c8 * 16, (T1 + 32 * (g >> 1)) * 1024 + cg * 128);
        }
        if (g == 2) {
          STAMP(28);
          lwait(CNT(cF + w), 4);
          STAMP(29);
#pragma unroll
          for (int n = 33; n < 41; ++n) h2_ld(n);
        }
      }
      STAMP(31);
    }
  } else {
    u64 tag;
    {
      const srd_t rm = __builtin_amdgcn_make_buffer_rsrc((void*)P.wmod, 0, 512 * 6144, 0x00020000);
      const srd_t rc = __builtin_amdgcn_make_buffer_rsrc((void*)P.c, 0, 1024, 0x00020000);
      const bool prod = wg < 192;
      const u32 kq = wg / 48, lc = wg - kq * 48, k0 = 128 * kq + 32 * w + 4 * (l >> 3);
      const u32x2 ep = __builtin_amdgcn_raw_buffer_load_b64(ws, 0, WS_EPOCH, SC1);
      const u32x2 cv = __builtin_amdgcn_raw_buffer_load_b64(rc, prod ? k0 * 2 : OOB, 0, 0);
      u32x4 wm[4];
#pragma unroll
      for (int i = 0; i < 4; ++i) wm[i] = __builtin_amdgcn_raw_buffer_load_b128(rm, prod ? (k0 + i) * 6144 + lc * 128 + (l & 7) * 16 : OOB, 0, 0);
      const u32 pre = __builtin_amdgcn_raw_buffer_load_b32(ws, l < 4 ? (4 * w + l) * 128 : OOB, WS_FLAG + wg * 2048, SC1);
      __builtin_amdgcn_sched_barrier(0);
      __builtin_amdgcn_s_barrier();
      asm volatile("" ::: "memory");
      u32x4 xr[5][4];
#pragma unroll
      for (int g = 0; g < 5; ++g)
#pragma unroll
        for (int i = 0; i < 4; ++i)
          xr[g][i] = __builtin_amdgcn_raw_buffer_load_b128(rx, (T1 - 16 + 16 * g + 4 * w + q) * 1024 + i * 256 + tok * 16, 0, 0);
      __builtin_amdgcn_sched_barrier(0);
      tag = ((u64)rfl(ep.x) | ((u64)rfl(ep.y) << 32)) + 0x40100401ull;
      if (prod) {
        float sc[4] = {bflo(cv.x), bfhi(cv.x), bflo(cv.y), bfhi(cv.y)}, a[8] = {};
#pragma unroll
        for (int i = 0; i < 4; ++i) {
          const float s = rbf(sc[i] * __builtin_amdgcn_rcpf(1.f + __builtin_amdgcn_exp2f(sc[i] * -1.4426950408889634f)));
#pragma unroll
          for (int j = 0; j < 4; ++j) { a[2 * j] = fmaf(s, bflo(wm[i][j]), a[2 * j]); a[2 * j + 1] = fmaf(s, bfhi(wm[i][j]), a[2 * j + 1]); }
        }
        float b[4], d[2];
#pragma unroll
        for (int j = 0; j < 4; ++j) { auto t = __builtin_amdgcn_permlane32_swap(uf(a[j]), uf(a[j + 4]), false, false); b[j] = fu(t[0]) + fu(t[1]); }
#pragma unroll
        for (int j = 0; j < 2; ++j) { auto t = __builtin_amdgcn_permlane16_swap(uf(b[j]), uf(b[j + 2]), false, false); d[j] = fu(t[0]) + fu(t[1]); }
        d[0] += dpp<0x128>(d[0]); d[1] += dpp<0x128>(d[1]);
        lds_st<f32x2>(L_RS + w * 256 + ((l & 7) * 8 + (l >> 5) * 4 + (q & 1) * 2) * 4, f32x2{d[0], d[1]});
        if (arrive_rtn(CNT(cM1)) == 3) {
          const u32 o = L_RS + tok * 16;
          const f32x4 s = (lds_ld<f32x4>(o) + lds_ld<f32x4>(o + 256)) + (lds_ld<f32x4>(o + 512) + lds_ld<f32x4>(o + 768));
          u32x4 e;
#pragma unroll
          for (int j = 0; j < 4; ++j) e[j] = ((uf(s[j]) + 512u) & ~1023u) | ((u32)(tag >> (10 * j)) & 1023u);
          st16_sc1(e, ws, l < 16 ? l * 16 : OOB, WS_MODP + kq * 12288 + lc * 256);
        }
      }
      asm volatile("" ::"v"(pre));
      STAMP(1);
      float rs[5], nm[5];
#pragma unroll
      for (int g = 0; g < 5; ++g) {
        float s = 0.f, ss = 0.f;
#pragma unroll
        for (int i = 0; i < 4; ++i)
#pragma unroll
          for (int j = 0; j < 4; ++j) {
            const u32 xw = xr[g][i][j];
            const bf16x2 v = __builtin_bit_cast(bf16x2, xw);
            s = __builtin_amdgcn_fdot2_f32_bf16(v, __builtin_bit_cast(bf16x2, 0x3f803f80u), s, false);
            ss = __builtin_amdgcn_fdot2_f32_bf16(v, v, ss, false);
          }
        s += dpp<0x128>(s); ss += dpp<0x128>(ss);
        s += dpp<0x124>(s); ss += dpp<0x124>(ss);
        s += dpp<0x122>(s); ss += dpp<0x122>(ss);
        s += dpp<0x121>(s); ss += dpp<0x121>(ss);
        const float m = s * (1.f / 512);
        rs[g] = __builtin_amdgcn_rsqf(fmaxf(ss * (1.f / 512) - m * m, 0.f) + 1e-5f);
        nm[g] = -m * rs[g];
      }
      STAMP(2);
      f32x4 mv = gather_mod(ws, 256 * w + 4 * l, tag);
      if (w >= 2) {
#pragma unroll
        for (int j = 0; j < 4; ++j) mv[j] = rbf(1.f + mv[j]);
      }
      lds_st<f32x4>(L_SB + w * 1024 + l * 16, mv);
      if (w == 0) lds_st<u32x2>(CNT(cTag), u32x2{(u32)tag, (u32)(tag >> 32)});
      arrive(CNT(cM2)); lwait(CNT(cM2), 4);
      STAMP(3);
      if (w == 0) gflag(ws, l == 0 ? WS_HELLO + wg * 8 : OOB, tag);
      f32x4 sh[4][2], sl[4][2];
      u32 ha[4];
#pragma unroll
      for (int i = 0; i < 4; ++i) {
#pragma unroll
        for (int e = 0; e < 2; ++e) { sh[i][e] = lds_ld<f32x4>(L_SB + i * 512 + tok * 32 + e * 16); sl[i][e] = lds_ld<f32x4>(L_SB + 2048 + i * 512 + tok * 32 + e * 16); }
        ha[i] = ((16 * i + tok - 8 * xcd) & 63) * 256 + (((4 * w + q) ^ (tok & 7)) << 4);
      }
#pragma unroll
      for (int g = 0; g < 5; ++g) {
#pragma unroll
        for (int i = 0; i < 4; ++i) {
          const u32x4 v = xr[g][i];
          const f32x4 n0 = f32x4{bflo(v.x), bfhi(v.x), bflo(v.y), bfhi(v.y)} * rs[g] + nm[g];
          const f32x4 n1 = f32x4{bflo(v.z), bfhi(v.z), bflo(v.w), bfhi(v.w)} * rs[g] + nm[g];
          const f32x4 h0 = n0 * sl[i][0] + sh[i][0], h1 = n1 * sl[i][1] + sh[i][1];
          lds_st<u32x4>(L_H + g * 16384 + ha[i], u32x4{cvtpk(h0[0], h0[1]), cvtpk(h0[2], h0[3]), cvtpk(h1[0], h1[1]), cvtpk(h1[2], h1[3])});
        }
        if (g > 0) arrive(CNT(cH + g - 1));
        if (g == 1) STAMP(4);
      }
      STAMP(5);
    }
    {
      lwait(CNT(cWc), 4);
      STAMP(6);
      f32x4 mv = gather_mod(ws, 1536 + 256 * w + 4 * l, tag);
      if (w >= 2) {
#pragma unroll
        for (int j = 0; j < 4; ++j) mv[j] = rbf(1.f + mv[j]);
      }
      const srd_t rw = __builtin_amdgcn_make_buffer_rsrc((void*)P.wo, 0, 512 * 1024, 0x00020000);
      const u32 ve = drow * 1024 + cg * 128 + dch0 * 16, vo = ve ^ 64;
      auto issue = [&](int i) {
        const u32 so = (4 * w + ((i + xcd) & 3)) * 32768, d = L_S + i * 16384 + w * 4096;
        dma(rw, d, ve, so); dma(rw, d + 1024, vo, so + 8192); dma(rw, d + 2048, ve, so + 16384); dma(rw, d + 3072, vo, so + 24576);
      };
      if (w == 0) {
        const f32x4 gv = gather_mod(ws, ((l & 16) ? 2560 : 1024) + 64 * cg + 4 * tok, tag);
        lds_st<f32x4>(L_G3 + ((l & 16) ? 512 : 0) + tok * 16, gv);
      }
      issue(3);
      lwait(CNT(cH + 3), 4);
      const u32 k = (256 * w + 4 * l) & 511;
      lds_st<u32x2>(L_SB + (w < 2 ? 1024 : 0) + ((k - 64 * xcd) & 511) * 2, u32x2{cvtpk(mv[0], mv[1]), cvtpk(mv[2], mv[3])});
      if (w >= 2 && (k >> 6) == cg) lds_st<f32x4>(L_G3 + 256 + (k & 63) * 4, mv);
      arrive(CNT(cM3));
      lwait(CNT(cWc), 8); issue(0);
      lwait(CNT(cWc), 12); issue(1); issue(2);
      STAMP(7);
    }
    u32x4 W[48];
    f32x4 uv[3] = {};
    {
      const float ninf = -__builtin_inff();
      const u32 par = tok & 1;
      const bool c0 = 4 * q + par <= tok, c1 = 4 * q + 2 + par <= tok;
      f32x4 bc, bp;
#pragma unroll
      for (int k = 0; k < 4; ++k) {
        const bool pm = (k & 1) == par, cu = k < 2 ? c0 : c1;
        bc[k] = pm && cu ? 0.f : ninf; bp[k] = pm && !cu ? 0.f : ninf;
      }
      const u32 s0 = par ? 0x01000c0cu : 0x0c0c0100u, s1 = par ? 0x03020c0cu : 0x0c0c0302u, zz = 0x0c0c0c0cu;
      const u32 sp0 = c0 ? zz : s0, sp1 = c1 ? zz : s1, sc0 = c0 ? s0 : zz, sc1 = c1 ? s1 : zz;
      const u32 dump = L_DUMP + w * 2560 + l * 8;
      u32x2 kp, vp, oa;
      auto attn = [&](int g) {
        lwait(CNT(cQ + w), g + 1);
        const u32x2 qv = lds_ld<u32x2>(dump), kc = lds_ld<u32x2>(dump + 512), vc = lds_ld<u32x2>(dump + 1024);
        f32x4 b2 = bp;
        if (g == 0) {
          kp = lds_ld<u32x2>(dump + 1536); vp = lds_ld<u32x2>(dump + 2048);
          if (T1 == 0) b2 = f32x4{ninf, ninf, ninf, ninf};
        }
        arrive(CNT(cQf + w));
        const f32x4 sc = mfma16(kc, qv, bc), sp = mfma16(kp, qv, b2);
        const float e0 = fmaxf(fmaxf(sc[0], sc[1]), fmaxf(sp[0], sp[1])), e1 = fmaxf(fmaxf(sc[2], sc[3]), fmaxf(sp[2], sp[3]));
        const float cs = 0.25f * 1.4426950408889634f, mx = max32(max16(fmaxf(e0, e1))) * cs;
        const float p0 = __builtin_amdgcn_exp2f(fmaf(e0, cs, -mx)), p1 = __builtin_amdgcn_exp2f(fmaf(e1, cs, -mx));
        const u32 pk = cvtpk(p0, p1);
        const u32x4 pb = {__builtin_amdgcn_perm(0u, pk, sp0), __builtin_amdgcn_perm(0u, pk, sp1), __builtin_amdgcn_perm(0u, pk, sc0), __builtin_amdgcn_perm(0u, pk, sc1)};
        const f32x4 o = mfma32(u32x4{vp.x, vp.y, vc.x, vc.y}, pb, f32x4{0.f, 0.f, 0.f, 0.f});
        const float inv = __builtin_amdgcn_rcpf(sum32(sum16(p0 + p1)));
        const u32x2 ob = {cvtpk(o[0] * inv, o[1] * inv), cvtpk(o[2] * inv, o[3] * inv)};
        kp = kc; vp = vc;
        if (!(g & 1)) oa = ob;
        else {
          auto ex = __builtin_amdgcn_permlane16_swap(oa.x, ob.x, false, false);
          auto ey = __builtin_amdgcn_permlane16_swap(oa.y, ob.y, false, false);
          st16_sc1(u32x4{ex[0], ey[0], ex[1], ey[1]}, ws, (q & 1) * 16384 + (q >> 1) * 256 + tok * 16,
                                                 WS_O + ((T1 >> 4) + g - 1) * 16384 + (4 * cg + w) * 512);
        }
      };
      auto publish_o = [&](int j) {
        if (arrive_rtn(CNT(cPo + j)) == 3)
          gflag(ws, l < 8 ? WS_FLAG + ((tq * 8 + l) * 8 + xcd) * 2048 + (cg >> 1) * 128 + j * 16 + (cg & 1) * 8 : OOB, tag);
      };
      if (fg != 15) {
        const srd_t rg = __builtin_amdgcn_make_buffer_rsrc((void*)P.wg, 0, 512 * 2752, 0x00020000);
        const srd_t ru = __builtin_amdgcn_make_buffer_rsrc((void*)P.wu, 0, 512 * 2752, 0x00020000);
        u32 gv[2];
        {
          const u32 row = 8 * w + q, o = (2752 * row + 192 * fg) & ~127u, sl = tok ^ (((row ^ fg) & 1) | (row & 2) | ((row >> 1) & 4));
          gv[0] = o + (sl << 4); gv[1] = o + ((sl ^ 8) << 4);
        }
        auto issue = [&](int ks) {
          const u32 so = ((ks + 2 * xcd) & 15) * (32 * 2752), d = L_H + (ks & 3) * 16384 + w * 2048;
          dma(rg, d, gv[0], so); dma(rg, d + 1024, gv[0], so + 11008); dma(ru, d + 8192, gv[1], so); dma(ru, d + 9216, gv[1], so + 11008);
        };
        u32 ga[3];
        {
          const u32 m = (l >> 1) & 1, pr = (jj ^ fg) & 1, gx = pr | (jj & 2) | ((q & 1) << 2) | (m << 3);
#pragma unroll
          for (int b = 0; b < 3; ++b) ga[b] = L_H + m * 8192 + (8 * q + jj) * 256 + (((3 * w + b + 4 * pr) ^ gx) << 4) + (l & 1) * 8;
        }
        const u32 sba = L_SB + (l & 1) * 1024 + q * 16;
        auto rd = [&](int ks) {
          const u32x4 sv = lds_ld<u32x4>(sba + ks * 64);
#pragma unroll
          for (int b = 0; b < 3; ++b) { W[ks * 3 + b] = tr_frag(ga[b] + (ks & 3) * 16384, 1024); uv[b] = mfma32(W[ks * 3 + b], sv, uv[b]); }
        };
        auto sync = [&](int ks) { arrive(CNT(cG)); lwait(CNT(cG), 4 * ks + 4); };
        attn(0); STAMP(8);
        lwait(CNT(cA), 4); issue(0); issue(1);
        attn(1); STAMP(9);
        lwait(CNT(cA + 1), 4); issue(2);
        vmwait<13>(); arrive(CNT(cWo + w));
        lwait(CNT(cM3), 4);
        vmwait<9>(); sync(0); rd(0);
        lwait(CNT(cA + 2), 4); issue(3);
        vmwait<8>(); publish_o(0); STAMP(10);
        attn(2);
        sync(1); issue(4); rd(1);
        vmwait<8>(); sync(2); issue(5); rd(2);
        attn(3); STAMP(11);
        vmwait<9>(); sync(3); issue(6); rd(3);
        vmwait<9>(); sync(4); issue(7); rd(4);
        vmwait<8>(); publish_o(1); STAMP(12);
        sync(5); issue(8); rd(5);
#pragma unroll
        for (int ks = 6; ks < 13; ++ks) { vmwait<8>(); sync(ks); issue(ks + 3); rd(ks); }
        STAMP(13);
        const u32 xv = (((l ^ (8 * (w & 1))) + 8 * xcd) & 63) * 16;
        auto x1_issue = [&](int s) {
          const u32 d = ((s & 1) ? L_X1 : L_X0) + w * 8192, so = WS_X1 + (T3 + 64 * (w >> 1) + 16 * s + 8 * (w & 1)) * 1024;
#pragma unroll
          for (int i = 0; i < 8; ++i) dma_sc1(ws, d + i * 1024, xv ^ (i << 4), so + i * 1024);
        };
        vmwait<8>(); sync(13); rd(13);
        if (w == 0) gpoll(ws, WS_FLAG + wg * 2048 + 4 * 128 + tok * 8, tag);
        vmwait<4>(); sync(14);
        STAMP(14);
        x1_issue(0); x1_issue(1);
        if (w == 0) arrive(CNT(cXs));
        rd(14);
        vmwait<16>(); sync(15); rd(15);
#pragma unroll
        for (int b = 0; b < 3; ++b) {
          if (tok < 2) lds_st<f32x4>(L_UV + w * 384 + b * 128 + tok * 64 + q * 16, (tok == 1 && q < 2) ? uv[b] * -1.4426950408889634f : uv[b]);
        }
        vmwait<8>(); arrive(CNT(cX));
        STAMP(15);
        u32 xa[4];
#pragma unroll
        for (int k = 0; k < 4; ++k) xa[k] = tok * 1024 + (((4 * k + q) ^ tok) << 4);
#pragma unroll 1
        for (u32 s = 0; s < 4; ++s) {
          lwait(CNT(cX + s), 4);
          const u32 xb = (s & 1) ? L_X1 : L_X0;
          f32x4 acc[2][3] = {};
#pragma unroll
          for (int ks = 0; ks < 16; ++ks) {
#pragma unroll
            for (int t = 0; t < 2; ++t) {
              const u32x4 a = lds_ld<u32x4>(xb + xa[ks & 3] + (ks >> 2) * 256 + t * 16384);
#pragma unroll
              for (int b = 0; b < 3; ++b) acc[t][b] = mfma32(W[ks * 3 + b], a, acc[t][b]);
            }
          }
          if (s == 0) { vmwait<0>(); arrive(CNT(cX + 1)); }
          if (s >= 2) lwait(CNT(cDf + w), s - 1);
          const u32 db = L_GD + (s & 1) * 24576 + w * 6144 + l * 16;
#pragma unroll
          for (int t = 0; t < 2; ++t)
#pragma unroll
            for (int b = 0; b < 3; ++b) lds_st<f32x4>(db + (t * 3 + b) * 1024, acc[t][b]);
          arrive(CNT(cB + s));
        }
        STAMP(20);
      } else {
        attn(0); attn(1);
        vmwait<1>(); arrive(CNT(cWo + w));
        vmwait<0>(); publish_o(0);
        attn(2); attn(3);
        vmwait<0>(); publish_o(1);
        if (wg == 255 && w == 0) {
#pragma unroll
          for (int i = 0; i < 4; ++i) gpoll(ws, WS_HELLO + i * 512 + l * 8, tag);
          gflag(ws, l == 0 ? WS_EPOCH : OOB, tag);
        }
      }
    }
    {
      const u32 cnt = w == 1 ? 5 : 4, e = l < cnt ? l : 0;
#pragma unroll 1
      for (u32 s = 0; s < 4; ++s) {
        gpoll(ws, WS_FLAG + wg * 2048 + (8 + 2 * w + (s >> 1)) * 128 + (s & 1) * 64 + e * 8, tag);
        arrive(CNT(cF + w));
      }
      STAMP(21);
    }
  }
}

extern "C" void kernel_launch(void* const* d_in, const int* in_sizes, int n_in, void* d_out, int out_size, void* d_ws, size_t ws_size,
                              hipStream_t stream) {
  Params p;
  p.x = d_in[0]; p.wqkv = d_in[1]; p.wo = d_in[2]; p.wg = d_in[4]; p.wu = d_in[5]; p.wd = d_in[6]; p.c = d_in[7]; p.wmod = d_in[8];
  p.out = d_out; p.ws = d_ws;
  adaln_block<<<256, 512, 0, stream>>>(p);
}
```

```cpp
#include <hip/hip_runtime.h>

#define DI __device__ __forceinline__
#define LAS __attribute__((address_space(3)))
typedef unsigned u32;
typedef unsigned long long u64;
typedef u32 u32x2 __attribute__((ext_vector_type(2)));
typedef u32 u32x4 __attribute__((ext_vector_type(4)));
typedef float f32x2 __attribute__((ext_vector_type(2)));
typedef float f32x4 __attribute__((ext_vector_type(4)));
typedef short s16x4 __attribute__((ext_vector_type(4)));
typedef __bf16 bf16x2 __attribute__((ext_vector_type(2)));
typedef __bf16 bf16x8 __attribute__((ext_vector_type(8)));
typedef __amdgpu_buffer_rsrc_t srd_t;

constexpr u32 WS_EPOCH = 0, WS_HELLO = 4096, WS_MODP = 8192, WS_FLAG = 65536, WS_STAT = 0x100000, WS_O = 0x200000,
              WS_X1 = 0x400000, WS_H2 = 0x600000, WS_STAMP = 0xC00000, WS_END = 0xD00000;
constexpr u32 H2_RB = 172 * 256;
constexpr u32 OOB = 0x80000000u;
constexpr int SC1 = 16;
constexpr u32 L_H = 0, L_S = 81920, L_DUMP = 147456, L_CNT = 157696, L_SB = 157952, L_RS = 162048, L_G3 = 163072;
constexpr u32 L_UV = L_SB + 2048, L_X0 = L_S + 32768, L_X1 = L_H, L_GD = L_H + 32768;
enum { cW = 0, cWc, cM1, cM2, cM3, cG, cR, cXs, cS, cH = 10, cPo = 12, cPx = 14, cA = 16, cB = 20, cX = 24, cPh = 28, cTag = 32,
       pQ = 0, pQf = 4, pWo = 8, pDf = 12, pF = 16 };
#define SPIN (1 << 16)
#define SLEEPSTR ""
constexpr float LOG2E = 1.4426950408889634f;

#define STAMP(k) do {} while (0)
#define SB() __builtin_amdgcn_sched_barrier(0)

DI u32 rfl(u32 x) { return __builtin_amdgcn_readfirstlane(x); }
DI u32 opq(u32 x) { asm volatile("" : "+v"(x)); return x; }
DI u32 lane_id() { u32 l; asm volatile("v_mbcnt_lo_u32_b32 %0, -1, 0\n\tv_mbcnt_hi_u32_b32 %0, -1, %0" : "=v"(l)); return l; }
DI float fu(u32 x) { return __builtin_bit_cast(float, x); }
DI u32 uf(float x) { return __builtin_bit_cast(u32, x); }
DI float bflo(u32 v) { return fu(v << 16); }
DI float bfhi(u32 v) { return fu(v & 0xffff0000u); }
DI u32 cvtpk(float lo, float hi) { f32x2 v = {lo, hi}; return __builtin_bit_cast(u32, __builtin_convertvector(v, bf16x2)); }
DI u32x2 pack4(f32x4 v) { return u32x2{cvtpk(v[0], v[1]), cvtpk(v[2], v[3])}; }
DI u32x4 pack8(f32x4 a, f32x4 b) { return u32x4{cvtpk(a[0], a[1]), cvtpk(a[2], a[3]), cvtpk(b[0], b[1]), cvtpk(b[2], b[3])}; }
DI f32x4 unp4(u32 a, u32 b) { return f32x4{bflo(a), bfhi(a), bflo(b), bfhi(b)}; }
DI float rbf(float x) { return bflo(cvtpk(x, x)); }
DI float dot2(u32 a, u32 b, float c) { return __builtin_amdgcn_fdot2_f32_bf16(__builtin_bit_cast(bf16x2, a), __builtin_bit_cast(bf16x2, b), c, false); }
DI f32x4 mfma32(u32x4 a, u32x4 b, f32x4 c) {
  return __builtin_amdgcn_mfma_f32_16x16x32_bf16(__builtin_bit_cast(bf16x8, a), __builtin_bit_cast(bf16x8, b), c, 0, 0, 0);
}
DI f32x4 mfma16(u32x2 a, u32x2 b, f32x4 c) {
  return __builtin_amdgcn_mfma_f32_16x16x16bf16_1k(__builtin_bit_cast(s16x4, a), __builtin_bit_cast(s16x4, b), c, 0, 0, 0);
}
template <int C> DI float dpp(float x) { return fu(__builtin_amdgcn_update_dpp(0, (int)uf(x), C, 0xf, 0xf, true)); }
DI float max2(float a, float b) { float r; asm("v_max_f32 %0, %1, %2" : "=v"(r) : "v"(a), "v"(b)); return r; }
DI float sum16(float x) { auto r = __builtin_amdgcn_permlane16_swap(uf(x), uf(x), false, false); return fu(r[0]) + fu(r[1]); }
DI float sum32(float x) { auto r = __builtin_amdgcn_permlane32_swap(uf(x), uf(x), false, false); return fu(r[0]) + fu(r[1]); }
DI float max16(float x) { auto r = __builtin_amdgcn_permlane16_swap(uf(x), uf(x), false, false); return fmaxf(fu(r[0]), fu(r[1])); }
DI float max32(float x) { auto r = __builtin_amdgcn_permlane32_swap(uf(x), uf(x), false, false); return fmaxf(fu(r[0]), fu(r[1])); }

template <class T> DI T lds_ld(u32 a) { return *(LAS T*)a; }
template <class T> DI void lds_st(u32 a, T v) { *(LAS T*)a = v; }
DI u32x4 tr_frag(u32 a, u32 hstep) {
  s16x4 lo = __builtin_amdgcn_ds_read_tr16_b64_v4i16((LAS s16x4*)a);
  s16x4 hi = __builtin_amdgcn_ds_read_tr16_b64_v4i16((LAS s16x4*)(a + hstep));
  u32x2 l = __builtin_bit_cast(u32x2, lo), h = __builtin_bit_cast(u32x2, hi);
  return u32x4{l.x, l.y, h.x, h.y};
}
DI void dma2(srd_t r, u32 lds, u32 v0, u32 v1, u32 so) {
  asm volatile("s_mov_b32 m0, %0\n\ts_nop 0\n\tbuffer_load_dwordx4 %1, %3, %4 offen lds\n\tbuffer_load_dwordx4 %2, %3, %4 offen offset:1024 lds"
               ::"s"(lds), "v"(v0), "v"(v1), "s"(r), "s"(so) : "memory");
}
DI void dma4(srd_t r, u32 lds, u32 v0, u32 v1, u32 s0, u32 s1) {
  asm volatile("s_mov_b32 m0, %0\n\ts_nop 0\n\tbuffer_load_dwordx4 %1, %3, %4 offen lds\n\tbuffer_load_dwordx4 %2, %3, %4 offen offset:1024 lds\n\t"
               "buffer_load_dwordx4 %1, %3, %5 offen offset:2048 lds\n\tbuffer_load_dwordx4 %2, %3, %5 offen offset:3072 lds"
               ::"s"(lds), "v"(v0), "v"(v1), "s"(r), "s"(s0), "s"(s1) : "memory");
}
DI void dma4_sc1(srd_t r, u32 lds, u32 v0, u32 so) {
  asm volatile("s_mov_b32 m0, %0\n\ts_nop 0\n\tbuffer_load_dwordx4 %1, %5, %6 offen sc1 lds\n\tbuffer_load_dwordx4 %2, %5, %6 offen offset:1024 sc1 lds\n\t"
               "buffer_load_dwordx4 %3, %5, %6 offen offset:2048 sc1 lds\n\tbuffer_load_dwordx4 %4, %5, %6 offen offset:3072 sc1 lds"
               ::"s"(lds), "v"(v0), "v"(v0 ^ 16), "v"(v0 ^ 32), "v"(v0 ^ 48), "s"(r), "s"(so) : "memory");
}
template <int N> DI void vmwait() { asm volatile("s_waitcnt vmcnt(%0)" ::"i"(N) : "memory"); }
DI void lgwait() { asm volatile("s_waitcnt lgkmcnt(0)" ::: "memory"); }
DI void st16_sc1(u32x4 v, srd_t r, u32 voff, u32 soff) {
  asm volatile("buffer_store_dwordx4 %0, %1, %2, %3 offen sc1\n\ts_nop 1" ::"v"(v), "v"(voff), "s"(r), "s"(soff) : "memory");
}
DI void st16(u32x4 v, srd_t r, u32 voff, u32 soff) {
  asm volatile("buffer_store_dwordx4 %0, %1, %2, %3 offen\n\ts_nop 1" ::"v"(v), "v"(voff), "s"(r), "s"(soff) : "memory");
}
DI void gflag(srd_t r, u32 voff, u64 tag) { __builtin_amdgcn_raw_buffer_store_b64(u32x2{(u32)tag, (u32)(tag >> 32)}, r, voff, 0, SC1); }

struct Sync {
  u32 cb, pb, one; int n;
  template <int I> DI void add(u32 a) {
    asm volatile("s_mov_b64 exec, 1\n\tds_add_u32 %0, %1 offset:%2\n\ts_mov_b64 exec, -1" ::"v"(a), "v"(one), "i"(4 * I) : "memory");
  }
  template <int I> DI u32 add_rtn(u32 a) {
    u32 v, s;
    asm volatile("s_mov_b64 exec, 1\n\tds_add_rtn_u32 %0, %2, %3 offset:%4\n\ts_mov_b64 exec, -1\n\ts_waitcnt lgkmcnt(0)\n\tv_readfirstlane_b32 %1, %0"
                 : "=&v"(v), "=s"(s) : "v"(a), "v"(one), "i"(4 * I) : "memory");
    return s;
  }
  template <int I> DI void wait(u32 a, u32 target) {
    u32 v, s;
    asm volatile("1:\n\tds_read_b32 %0, %3 offset:%5\n\ts_waitcnt lgkmcnt(0)\n\tv_readfirstlane_b32 %1, %0\n\ts_cmp_ge_u32 %1, %4\n\ts_cbranch_scc1 2f\n\t"
                 SLEEPSTR "s_sub_i32 %2, %2, 1\n\ts_cmp_gt_i32 %2, 0\n\ts_cbranch_scc1 1b\n2:"
                 : "=&v"(v), "=&s"(s), "+s"(n) : "v"(a), "s"(target), "i"(4 * I) : "memory", "scc");
  }
  DI void poll(srd_t r, u32 voff, u64 tag) {
    u64 v;
    asm volatile("1:\n\tbuffer_load_dwordx2 %0, %2, %3, 0 offen sc1\n\ts_waitcnt vmcnt(0)\n\tv_cmp_ne_u64 vcc, %4, %0\n\ts_cmp_eq_u64 vcc, 0\n\t"
                 "s_cbranch_scc1 2f\n\ts_sub_i32 %1, %1, 8\n\ts_cmp_gt_i32 %1, 0\n\ts_cbranch_scc1 1b\n2:"
                 : "=&v"(v), "+s"(n) : "v"(voff), "s"(r), "s"(tag) : "memory", "scc", "vcc");
  }
  DI f32x4 gather_mod(srd_t ws, u32 col, u64 tag) {
    const u32 t0 = tag & 1023, t1 = (tag >> 10) & 1023, t2 = (tag >> 20) & 1023, t3 = (tag >> 30) & 1023;
    u32x4 p[4];
    do {
      asm volatile("" ::: "memory");
      u32 bad = 0;
#pragma unroll
      for (int k = 0; k < 4; ++k) p[k] = __builtin_amdgcn_raw_buffer_load_b128(ws, col * 4, WS_MODP + k * 12288, SC1);
      SB();
#pragma unroll
      for (int k = 0; k < 4; ++k) bad |= (p[k].x ^ t0) | (p[k].y ^ t1) | (p[k].z ^ t2) | (p[k].w ^ t3);
      if (__builtin_amdgcn_ballot_w64((bad & 1023) != 0) == 0) break;
      n -= 8;
    } while (n > 0);
    f32x4 r;
#pragma unroll
    for (int j = 0; j < 4; ++j) r[j] = rbf((fu(p[0][j] & ~1023u) + fu(p[1][j] & ~1023u)) + (fu(p[2][j] & ~1023u) + fu(p[3][j] & ~1023u)));
    return r;
  }
};
#define ARR(i) sy.add<i>(sy.cb)
#define ARRP(i) sy.add<i>(sy.pb)
#define LAST(i) (sy.add_rtn<i>(sy.cb) == 3)
#define WAIT(i, t) sy.wait<i>(sy.cb, t)
#define WAITP(i, t) sy.wait<i>(sy.pb, t)

struct Params {
  const void *x, *wqkv, *wo, *wg, *wu, *wd, *c, *wmod;
  void *out, *ws;
};

__global__ void __launch_bounds__(512, 2) adaln_block(Params P) {
  __shared__ __attribute__((aligned(16))) char smem[163840];
  const u32 wave = rfl(threadIdx.x >> 6), w = wave & 3;
  const u32 wg = blockIdx.x, xcd = wg & 7, r = wg >> 3, tq = r >> 3, cg = r & 7, fg = r & 15;
  const u32 T1 = xcd * 256 + tq * 64, T3 = xcd * 256 + (r >> 4) * 128;
  asm volatile("" ::"s"((u32)(size_t)(LAS char*)smem) : "memory");
  STAMP(0);
  const srd_t ws = __builtin_amdgcn_make_buffer_rsrc(P.ws, 0, WS_END, 0x00020000);
  const srd_t rx = __builtin_amdgcn_make_buffer_rsrc((void*)P.x, 0, 2048 * 1024, 0x00020000);
  const f32x4 z4 = {0.f, 0.f, 0.f, 0.f};
  Sync sy;
  sy.cb = opq(L_CNT); sy.pb = opq(L_CNT + 160 + 4 * w); sy.one = opq(1); sy.n = SPIN;
  auto x1_issue = [&](int s) {
    const u32 l = lane_id(), xv = fg == 15 ? OOB : (((l ^ (8 * (w & 1))) + 8 * xcd) & 63) * 16;
    const u32 d = ((s & 1) ? L_X1 : L_X0) + w * 8192, so = WS_X1 + (T3 + 64 * (w >> 1) + 16 * s + 8 * (w & 1)) * 1024;
    dma4_sc1(ws, d, xv, so); dma4_sc1(ws, d + 4096, xv ^ 64, so + 4096);
  };

  if (wave < 4) {
    u32x4 W[48];
    f32x4 x1r[2];
    u64 tag;
    {
      const u32 l = lane_id(), q = l >> 4, tok = l & 15, jj = (l >> 2) & 3, drow = l >> 3;
      if (wave == 0) lds_st<u32>(L_CNT + 4 * l, 0u);
      const srd_t rw = __builtin_amdgcn_make_buffer_rsrc((void*)P.wqkv, 0, 512 * 3072, 0x00020000);
      const u32 v0 = opq((16 * w + drow) * 3072 + cg * 128 + ((l & 7) ^ (drow & 2)) * 16), v1 = opq((v0 ^ 64) + 8 * 3072 - 1024);
      const u32 tb = opq(L_S + (8 * q + jj) * 128 + (l & 3) * 8 + ((w ^ (jj >> 1) ^ ((q & 1) << 1)) << 5)), tb2 = opq(tb + 49152);
      const u32 he = opq(q * 256 + ((tok ^ q) << 4)), dump = opq(L_DUMP + w * 2560 + l * 8);
      lgwait();
      __builtin_amdgcn_s_barrier();
      asm volatile("" ::: "memory");
      auto issue = [&](int c) {
        const u32 so = ((c + xcd) & 7) * (64 * 3072), d = L_S + (c % 3) * 24576 + w * 2048;
#pragma unroll
        for (int s = 0; s < 3; ++s) dma2(rw, d + s * 8192, v0, v1, so + s * 1024);
      };
      auto rd = [&](int c) {
#pragma unroll
        for (int kk = 0; kk < 2; ++kk)
#pragma unroll
          for (int s = 0; s < 3; ++s) W[(2 * c + kk) * 3 + s] = tr_frag((c % 3 == 2 ? tb2 : tb + (c % 3) * 24576) + s * 8192 + kk * 4096, 512);
      };
      issue(0); issue(1); issue(2);
      vmwait<12>(); ARR(cW); WAIT(cW, 4); rd(0);
      STAMP(1);
#pragma unroll
      for (int c = 1; c < 6; ++c) { vmwait<6>(); ARR(cW); WAIT(cW, 4 * c + 4); issue(c + 2); rd(c); }
      ARR(cWc);
      STAMP(2);
      u32 e0 = he, o0 = (he ^ 64) + 1024;
      auto hf = [&](int off, int ks) { return lds_ld<u32x4>(((ks & 1) ? o0 : e0) + off + (ks >> 1) * 2048); };
      WAIT(cH, 4);
      __builtin_amdgcn_s_setprio(2);
      STAMP(3);
      u32x4 a[4];
      {
        f32x4 aq = z4, ak = z4, av = z4, hk = z4, hv = z4;
        a[0] = hf(16384, 0); a[1] = hf(0, 0); a[2] = hf(16384, 1); a[3] = hf(0, 1);
        SB();
#pragma unroll
        for (int ks = 0; ks < 16; ++ks) {
          if (ks == 6) { vmwait<6>(); ARR(cW); WAIT(cW, 28); rd(6); ARR(cWc); SB(); }
          if (ks == 12) { vmwait<0>(); ARR(cW); WAIT(cW, 32); rd(7); ARR(cWc); WAIT(cH + 1, 4); STAMP(4); SB(); }
          const int b = (ks & 1) * 2;
          aq = mfma32(W[ks * 3], a[b], aq); ak = mfma32(W[ks * 3 + 1], a[b], ak); av = mfma32(a[b], W[ks * 3 + 2], av);
          a[b] = ks < 14 ? hf(16384, ks + 2) : hf(32768, b);
          SB();
          hk = mfma32(W[ks * 3 + 1], a[b + 1], hk); hv = mfma32(a[b + 1], W[ks * 3 + 2], hv);
          a[b + 1] = ks < 14 ? hf(0, ks + 2) : hf(32768, b + 1);
          SB();
        }
        lds_st<u32x2>(dump, pack4(aq)); lds_st<u32x2>(dump + 512, pack4(ak)); lds_st<u32x2>(dump + 1024, pack4(av));
        lds_st<u32x2>(dump + 1536, pack4(hk)); lds_st<u32x2>(dump + 2048, pack4(hv));
        ARRP(pQ); ARR(cA);
      }
      STAMP(5);
      u32 ca = sy.cb;
#pragma unroll 1
      for (u32 g = 1; g < 4; ++g) {
        f32x4 aq = z4, ak = z4, av = z4;
        e0 += 16384; o0 += 16384; ca += 4;
#pragma unroll
        for (int ks = 0; ks < 16; ++ks) {
          aq = mfma32(W[ks * 3], a[ks & 3], aq); ak = mfma32(W[ks * 3 + 1], a[ks & 3], ak); av = mfma32(a[ks & 3], W[ks * 3 + 2], av);
          a[ks & 3] = hf(16384, ks + 4);
          SB();
        }
        WAITP(pQf, g);
        lds_st<u32x2>(dump, pack4(aq)); lds_st<u32x2>(dump + 512, pack4(ak)); lds_st<u32x2>(dump + 1024, pack4(av));
        ARRP(pQ); sy.add<cA>(ca);
      }
      __builtin_amdgcn_s_setprio(0);
      STAMP(6);
      const u32x2 tg = lds_ld<u32x2>(L_CNT + 4 * cTag);
      tag = (u64)rfl(tg.x) | ((u64)rfl(tg.y) << 32);
    }
    {
      const u32 l = lane_id(), q = l >> 4, tok = l & 15, jj = (l >> 2) & 3;
      const u32 c8 = l & 7, rtk = 8 * (w & 1) + (l >> 3);
      const u32 ev = opq((8 * w + (l >> 3)) * 1024 + c8 * 16);
      u32x4 xres[2];
#pragma unroll
      for (int j = 0; j < 2; ++j) xres[j] = __builtin_amdgcn_raw_buffer_load_b128(rx, ev, (T1 + 32 * j) * 1024 + cg * 128, 0);
      SB();
      const u32 tb = L_S + w * 4096 + (8 * q + jj) * 128 + (l & 3) * 8 + (((jj >> 1) ^ ((q & 1) << 1)) << 5);
      WAITP(pWo, 1);
#pragma unroll
      for (int cb = 0; cb < 4; ++cb) {
        const u32 b = opq(tb ^ (cb << 5));
#pragma unroll
        for (int i = 0; i < 4; ++i) W[i * 4 + cb] = tr_frag(b + i * 16384, 512);
      }
      ARR(cR);
      const u32 wa = opq(L_S + w * 4096 + tok * 256 + ((q ^ (tok & 7)) << 4)), wb = opq(wa ^ 64);
      const u32 r0 = opq(L_S + (w >> 1) * 16384 + rtk * 256 + (((2 * c8) ^ (rtk & 7)) << 4)), r1 = opq(r0 ^ 16);
      const u32 l16 = opq(l * 16), sv = opq(c8 == 0 ? rtk * 8 : OOB), pv = opq(WS_FLAG + wg * 2048 + w * 128 + (l & 1) * 8);
      const u32 fv = opq(l < 16 ? WS_FLAG + (((r & 16) + l) * 8 + xcd) * 2048 + 4 * 128 + fg * 8 : OOB);
      WAIT(cM3, 4);
      const u32 g3 = L_G3 + c8 * 32;
      const f32x4 ga0 = lds_ld<f32x4>(g3), ga1 = lds_ld<f32x4>(g3 + 16), s0 = lds_ld<f32x4>(g3 + 256), s1 = lds_ld<f32x4>(g3 + 272);
      WAITP(pQf, 4);
      STAMP(7);
#pragma unroll
      for (int j = 0; j < 2; ++j) {
        sy.poll(ws, pv + j * 16, tag);
        if (j == 0) STAMP(8); else STAMP(12);
        u32x4 a[2][4];
#pragma unroll
        for (int i = 0; i < 4; ++i)
#pragma unroll
          for (int rb = 0; rb < 2; ++rb)
            a[rb][i] = __builtin_amdgcn_raw_buffer_load_b128(ws, l16, WS_O + ((T1 >> 4) + 2 * j + rb) * 16384 + (4 * w + ((i + xcd) & 3)) * 1024, SC1);
        SB();
        WAIT(cR, 4 + 8 * j);
        f32x4 acc[2][4] = {};
#pragma unroll
        for (int i = 0; i < 4; ++i)
#pragma unroll
          for (int rb = 0; rb < 2; ++rb) {
#pragma unroll
            for (int cb = 0; cb < 4; ++cb) acc[rb][cb] = mfma32(W[i * 4 + cb], a[rb][i], acc[rb][cb]);
            SB();
          }
#pragma unroll
        for (int rb = 0; rb < 2; ++rb) {
          lds_st<f32x4>(wa + rb * 16384, acc[rb][0]); lds_st<f32x4>(wb + rb * 16384, acc[rb][1]);
          lds_st<f32x4>(wa + rb * 16384 + 128, acc[rb][2]); lds_st<f32x4>(wb + rb * 16384 + 128, acc[rb][3]);
        }
        ARR(cR);
        if (j == 0) STAMP(9); else STAMP(13);
        WAIT(cR, 8 + 8 * j);
        const f32x4 y0 = (lds_ld<f32x4>(r0) + lds_ld<f32x4>(r0 + 4096)) + (lds_ld<f32x4>(r0 + 8192) + lds_ld<f32x4>(r0 + 12288));
        const f32x4 y1 = (lds_ld<f32x4>(r1) + lds_ld<f32x4>(r1 + 4096)) + (lds_ld<f32x4>(r1 + 8192) + lds_ld<f32x4>(r1 + 12288));
        ARR(cR);
        const f32x4 u0 = unp4(xres[j].x, xres[j].y) + ga0 * y0, u1 = unp4(xres[j].z, xres[j].w) + ga1 * y1;
        st16_sc1(pack8(u0 * s0, u1 * s1), ws, ev, WS_X1 + (T1 + 32 * j) * 1024 + cg * 128);
        const f32x4 t1 = u0 + u1, t2 = u0 * u0 + u1 * u1;
        float sm = (t1[0] + t1[1]) + (t1[2] + t1[3]), sq = (t2[0] + t2[1]) + (t2[2] + t2[3]);
        sm += dpp<0xB1>(sm); sq += dpp<0xB1>(sq);
        sm += dpp<0x4E>(sm); sq += dpp<0x4E>(sq);
        sm += dpp<0x141>(sm); sq += dpp<0x141>(sq);
        __builtin_amdgcn_raw_buffer_store_b64(u32x2{uf(sm), uf(sq)}, ws, sv, WS_STAT + (((T1 >> 4) + 2 * j + (w >> 1)) * 8 + cg) * 128, SC1);
        if (j == 0) { lds_st<f32x4>(L_DUMP + w * 2560 + l16, u0); lds_st<f32x4>(L_DUMP + w * 2560 + 1024 + l16, u1); }
        else { x1r[0] = u0; x1r[1] = u1; }
        if (j == 0) STAMP(10); else STAMP(14);
        vmwait<0>();
        if (j == 0 ? LAST(cPx) : LAST(cPx + 1)) gflag(ws, fv + j * 128, tag);
        if (j == 0) STAMP(11); else STAMP(15);
      }
    }
    {
      const srd_t rd = __builtin_amdgcn_make_buffer_rsrc((void*)P.wd, 0, 1376 * 1024, 0x00020000);
      auto kstep = [&](int i) { const u32 a = i + xcd, b = a - 11; return 11 * w + (a < b ? a : b); };
      auto wd_issue = [&](int rnd) {
        const u32 l = lane_id(), v0 = (l >> 3) * 1024 + cg * 128 + ((l & 7) ^ ((l >> 3) & 2)) * 16;
#pragma unroll
        for (int i = 2 * rnd; i < 2 * rnd + 2 && i < 11; ++i) {
          const u32 ks = kstep(i), ve = ks > 42 ? OOB : v0, so = ks > 42 ? 0 : ks * 32768;
          dma4(rd, L_S + w * 8192 + (i & 1) * 4096, ve, (ve ^ 64) + 7168, so, so + 14336);
        }
      };
      auto wd_read = [&](int rnd) {
        const u32 l = lane_id(), q = l >> 4, jj = (l >> 2) & 3;
        const u32 b0 = L_S + w * 8192 + (8 * q + jj) * 128 + (l & 3) * 8 + (((jj >> 1) ^ ((q & 1) << 1)) << 5);
#pragma unroll
        for (int cb = 0; cb < 4; ++cb) {
          const u32 b = opq(b0 ^ (cb << 5));
#pragma unroll
          for (int i = 2 * rnd; i < 2 * rnd + 2 && i < 11; ++i) W[i * 4 + cb] = tr_frag(b + (i & 1) * 4096, 512);
        }
        lgwait();
      };
      auto stats = [&](int j) {
        const u32 l = lane_id();
        u32x4 p = __builtin_amdgcn_raw_buffer_load_b128(ws, l * 16, WS_STAT + ((T3 >> 4) + 4 * (w >> 1) + 2 * j + (w & 1)) * 1024, SC1);
        float v[4];
#pragma unroll
        for (int k = 0; k < 4; ++k) { float t = fu(p[k]); t += dpp<0x128>(t); v[k] = sum32(sum16(t)); }
        const float m0 = v[0] * (1.f / 512), m1 = v[2] * (1.f / 512);
        const float r0 = __builtin_amdgcn_rsqf(fmaxf(v[1] * (1.f / 512) - m0 * m0, 0.f) + 1e-5f);
        const float r1 = __builtin_amdgcn_rsqf(fmaxf(v[3] * (1.f / 512) - m1 * m1, 0.f) + 1e-5f);
        lds_st<f32x4>(L_RS + ((2 * j + (w & 1)) * 2 + (w >> 1)) * 128 + (l & 7) * 16, f32x4{r0, m0, r1, m1});
        ARR(cS);
      };
      auto glu = [&](f32x4 g, f32x4 u, u32 uvb, float rs, float mu) {
        const f32x4 ug = lds_ld<f32x4>(uvb), uu = lds_ld<f32x4>(uvb + 32), vg = lds_ld<f32x4>(uvb + 64), vu = lds_ld<f32x4>(uvb + 96);
        const float rn = rs * -LOG2E;
        f32x4 h;
#pragma unroll
        for (int k = 0; k < 4; ++k) {
          const float e = __builtin_amdgcn_exp2f(fmaf(rn, fmaf(-mu, ug[k], g[k]), vg[k]));
          h[k] = fmaf(rs, fmaf(-mu, uu[k], u[k]), vu[k]) * __builtin_amdgcn_rcpf(1.f + e);
        }
        return pack4(h);
      };
      u32 d01, d2, uv01, uv2, sta, hv0, hv1;
      {
        const u32 l = lane_id(), ti = l >> 5, bq = (l >> 4) & 1, tok = l & 15, db = L_GD + w * 6144 + ti * 3072 + tok * 16;
        const u32 pc = 12 * fg + 3 * w, vb = ti * (4 * H2_RB) + tok * 16;
        d01 = opq(db + bq * 1024); d2 = opq(db + 2048 + bq * 256); uv01 = opq(L_UV + w * 384 + bq * 128); uv2 = opq(L_UV + w * 384 + 256 + bq * 16);
        sta = opq(L_RS + ti * 128 + tok * 8);
        hv0 = opq(pc + bq < 172 ? vb + (pc + bq) * 256 : OOB); hv1 = opq((bq == 0 && pc + 2 < 172) ? vb + (pc + 2) * 256 : OOB);
      }
      auto epilogue = [&](int s) {
        const u32 o = (s & 1) * 24576, so = WS_H2 + ((T3 >> 4) + s) * H2_RB;
        const f32x2 st = lds_ld<f32x2>(sta + s * 256);
        const f32x4 g0 = lds_ld<f32x4>(d01 + o), u0 = lds_ld<f32x4>(d01 + o + 512), g1 = lds_ld<f32x4>(d01 + o + 256), u1 = lds_ld<f32x4>(d01 + o + 768);
        const u32x2 h0 = glu(g0, u0, uv01, st[0], st[1]);
        SB();
        const f32x4 g2 = lds_ld<f32x4>(d2 + o), u2 = lds_ld<f32x4>(d2 + o + 512);
        const u32x2 h1 = glu(g1, u1, uv01 + 16, st[0], st[1]);
        st16_sc1(u32x4{h0.x, h0.y, h1.x, h1.y}, ws, hv0, so);
        ARRP(pDf);
        const u32x2 h2 = glu(g2, u2, uv2, st[0], st[1]);
        auto ex = __builtin_amdgcn_permlane16_swap(h2.x, h2.x, false, false);
        auto ey = __builtin_amdgcn_permlane16_swap(h2.y, h2.y, false, false);
        st16_sc1(u32x4{ex[0], ey[0], ex[1], ey[1]}, ws, hv1, so);
      };
      auto flag_h2 = [&](int s) {
        const u32 l = lane_id(), w1 = (12 * fg) / 44, w2 = (12 * fg + 11) / 44, wc = l < 16 ? w1 : w2, first = wc == 0 ? 0 : 4 * wc - 1;
        const bool on = (l < 16 || (l < 32 && w2 != w1)) && wc < 4;
        gflag(ws, on ? WS_FLAG + (((r & 16) + (l & 15)) * 8 + xcd) * 2048 + (8 + 2 * wc + (s >> 1)) * 128 + (s & 1) * 64 + (fg - first) * 8 : OOB, tag);
      };
      WAIT(cR, 20);
      WAIT(cXs, 1);
      STAMP(16);
      stats(0);
      wd_issue(0);
      if (w == 0) { sy.poll(ws, WS_FLAG + wg * 2048 + 5 * 128 + (lane_id() & 15) * 8, tag); ARR(cXs); }
      vmwait<0>(); wd_read(0); wd_issue(1);
      WAIT(cXs, 2);
      stats(1);
      vmwait<0>(); wd_read(1); wd_issue(2);
      WAIT(cS, 8);
      STAMP(17);
      WAIT(cB, 4); STAMP(18); x1_issue(2); epilogue(0);
      vmwait<10>(); wd_read(2); wd_issue(3);
      vmwait<8>(); ARR(cX + 2); if (LAST(cPh)) flag_h2(0);
      STAMP(19);
      vmwait<0>(); wd_read(3); wd_issue(4);
      WAIT(cB + 1, 4); STAMP(20); x1_issue(3); epilogue(1);
      vmwait<10>(); wd_read(4); wd_issue(5);
      vmwait<4>(); ARR(cX + 3); if (LAST(cPh + 1)) flag_h2(1);
      STAMP(21);
      vmwait<0>(); wd_read(5);
      WAIT(cB + 2, 4); STAMP(22); epilogue(2);
      vmwait<0>(); if (LAST(cPh + 2)) flag_h2(2);
      STAMP(23);
      WAIT(cB + 3, 4); STAMP(24); epilogue(3);
      STAMP(25);
      const srd_t ro = __builtin_amdgcn_make_buffer_rsrc(P.out, 0, 2048 * 1024, 0x00020000);
      const u32 l = lane_id(), l16 = opq(l * 16);
      u32x4 a[8];
      auto h2_ld = [&](int n) {
        const u32 ks = kstep(n % 11);
        a[n & 7] = __builtin_amdgcn_raw_buffer_load_b128(ws, l16, WS_H2 + ((T1 >> 4) + n / 11) * H2_RB + (ks > 42 ? 42 : ks) * 1024, SC1);
      };
      WAITP(pF, 1);
#pragma unroll
      for (int n = 0; n < 8; ++n) h2_ld(n);
      vmwait<8>(); if (LAST(cPh + 3)) flag_h2(3);
      STAMP(26);
      const u32 c8 = l & 7, rtk = 8 * (w & 1) + (l >> 3);
      const u32 wa = opq(L_X0 + w * 4096 + (l & 15) * 256 + (((l >> 4) ^ (l & 7)) << 4));
      const u32 r0 = opq(L_X0 + (w >> 1) * 16384 + rtk * 256 + (((2 * c8) ^ (rtk & 7)) << 4));
#pragma unroll
      for (int g = 0; g < 4; ++g) {
        f32x4 acc[4] = {};
        if (g < 2) WAITP(pF, g + 2);
        SB();
#pragma unroll
        for (int i = 0; i < 11; ++i) {
          const int n = 11 * g + i;
#pragma unroll
          for (int cb = 0; cb < 4; ++cb) acc[cb] = mfma32(W[i * 4 + cb], a[n & 7], acc[cb]);
          if (n + 8 < 33 || (g == 3 && n + 8 < 44)) h2_ld(n + 8);
          SB();
        }
        {
          const u32 wb = wa ^ 64, o = (g & 1) * 16384;
          lds_st<f32x4>(wa + o, acc[0]); lds_st<f32x4>(wb + o, acc[1]); lds_st<f32x4>(wa + o + 128, acc[2]); lds_st<f32x4>(wb + o + 128, acc[3]);
        }
        ARR(cR);
        if (g == 3) STAMP(30);
        WAIT(cR, 24 + 4 * g);
        if ((w >> 1) == (g & 1)) {
          const u32 r1 = r0 ^ 16, gb = opq(L_G3 + 512 + (l16 & 112) * 2), xa = opq(L_DUMP + w * 2560 + l16);
          const f32x4 y0 = (lds_ld<f32x4>(r0) + lds_ld<f32x4>(r0 + 4096)) + (lds_ld<f32x4>(r0 + 8192) + lds_ld<f32x4>(r0 + 12288));
          const f32x4 o0 = (g < 2 ? lds_ld<f32x4>(xa) : x1r[0]) + lds_ld<f32x4>(gb) * y0;
          if (g < 2) SB();
          const f32x4 y1 = (lds_ld<f32x4>(r1) + lds_ld<f32x4>(r1 + 4096)) + (lds_ld<f32x4>(r1 + 8192) + lds_ld<f32x4>(r1 + 12288));
          const f32x4 o1 = (g < 2 ? lds_ld<f32x4>(xa + 1024) : x1r[1]) + lds_ld<f32x4>(gb + 16) * y1;
          st16(pack8(o0, o1), ro, (l16 >> 7) * 1024 + (l16 & 112), (T1 + 32 * (g >> 1) + 8 * w) * 1024 + cg * 128);
        }
        if (g == 2) {
          STAMP(28);
          WAITP(pF, 4);
          STAMP(29);
#pragma unroll
          for (int n = 33; n < 41; ++n) h2_ld(n);
        }
      }
      STAMP(31);
    }
  } else {
    u64 tag;
    {
      __builtin_amdgcn_s_setprio(3);
      const u32 l = lane_id(), q = l >> 4, tok = l & 15;
      const srd_t rm = __builtin_amdgcn_make_buffer_rsrc((void*)P.wmod, 0, 512 * 6144, 0x00020000);
      const srd_t rc = __builtin_amdgcn_make_buffer_rsrc((void*)P.c, 0, 1024, 0x00020000);
      const u32 kq = wg / 48, lc = wg - kq * 48, k0 = 128 * kq + 32 * w + 4 * (l >> 3);
      u32x4 wm[4], xr[5][4];
      const u32 mv0 = k0 * 6144 + lc * 128 + (l & 7) * 16;
#pragma unroll
      for (int i = 0; i < 4; ++i) wm[i] = __builtin_amdgcn_raw_buffer_load_b128(rm, mv0 + i * 6144, 0, 0);
      const u32x2 cv = __builtin_amdgcn_raw_buffer_load_b64(rc, k0 * 2, 0, 0);
      const u32x2 ep = __builtin_amdgcn_raw_buffer_load_b64(ws, 0, WS_EPOCH, SC1);
      const u32 pre = __builtin_amdgcn_raw_buffer_load_b32(ws, l < 4 ? (4 * w + l) * 128 : OOB, WS_FLAG + wg * 2048, SC1);
      const u32 xo = (T1 - 16 + 4 * w + q) * 1024 + tok * 16;
      auto xld = [&](int g) {
        const u32 o = opq(xo + g * 16384);
#pragma unroll
        for (int i = 0; i < 4; ++i) xr[g][i] = __builtin_amdgcn_raw_buffer_load_b128(rx, o + i * 256, 0, 0);
      };
      xld(0);
      SB();
      STAMP(30);
      __builtin_amdgcn_s_barrier();
      asm volatile("" ::: "memory");
      SB();
      tag = ((u64)rfl(ep.x) | ((u64)rfl(ep.y) << 32)) + 0x40100401ull;
      STAMP(31);
      if (kq < 4) {
        float sc[4] = {bflo(cv.x), bfhi(cv.x), bflo(cv.y), bfhi(cv.y)}, a[8] = {};
#pragma unroll
        for (int i = 0; i < 4; ++i) {
          const float s = rbf(sc[i] * __builtin_amdgcn_rcpf(1.f + __builtin_amdgcn_exp2f(sc[i] * -LOG2E)));
#pragma unroll
          for (int j = 0; j < 4; ++j) { a[2 * j] = fmaf(s, bflo(wm[i][j]), a[2 * j]); a[2 * j + 1] = fmaf(s, bfhi(wm[i][j]), a[2 * j + 1]); }
        }
        float b[4], d[2];
#pragma unroll
        for (int j = 0; j < 4; ++j) { auto t = __builtin_amdgcn_permlane32_swap(uf(a[j]), uf(a[j + 4]), false, false); b[j] = fu(t[0]) + fu(t[1]); }
#pragma unroll
        for (int j = 0; j < 2; ++j) { auto t = __builtin_amdgcn_permlane16_swap(uf(b[j]), uf(b[j + 2]), false, false); d[j] = fu(t[0]) + fu(t[1]); }
        d[0] += dpp<0x128>(d[0]); d[1] += dpp<0x128>(d[1]);
        lds_st<f32x2>(L_RS + w * 256 + ((l & 7) * 8 + (l >> 5) * 4 + (q & 1) * 2) * 4, f32x2{d[0], d[1]});
        STAMP(32);
        if (LAST(cM1)) {
          const u32 o = L_RS + tok * 16;
          const f32x4 s = (lds_ld<f32x4>(o) + lds_ld<f32x4>(o + 256)) + (lds_ld<f32x4>(o + 512) + lds_ld<f32x4>(o + 768));
          u32x4 e;
#pragma unroll
          for (int j = 0; j < 4; ++j) e[j] = ((uf(s[j]) + 512u) & ~1023u) | ((u32)(tag >> (10 * j)) & 1023u);
          st16_sc1(e, ws, l < 16 ? l * 16 : OOB, WS_MODP + kq * 12288 + lc * 256);
        }
      }
      asm volatile("" ::"v"(pre));
      STAMP(1);
      xld(1); xld(2); xld(3); xld(4);
      SB();
      STAMP(33);
      float rs[5], nm[5];
#pragma unroll
      for (int g = 0; g < 5; ++g) {
        float s = 0.f, ss = 0.f;
#pragma unroll
        for (int i = 0; i < 4; ++i)
#pragma unroll
          for (int j = 0; j < 4; ++j) { s = dot2(xr[g][i][j], 0x3f803f80u, s); ss = dot2(xr[g][i][j], xr[g][i][j], ss); }
        s += dpp<0x128>(s); ss += dpp<0x128>(ss);
        s += dpp<0x124>(s); ss += dpp<0x124>(ss);
        s += dpp<0x122>(s); ss += dpp<0x122>(ss);
        s += dpp<0x121>(s); ss += dpp<0x121>(ss);
        const float m = s * (1.f / 512);
        rs[g] = __builtin_amdgcn_rsqf(fmaxf(ss * (1.f / 512) - m * m, 0.f) + 1e-5f);
        nm[g] = fu(opq(uf(-m * rs[g])));
      }
      u32 ha[4];
#pragma unroll
      for (int i = 0; i < 4; ++i) ha[i] = opq(((16 * i + tok - 8 * xcd) & 63) * 256 + (((4 * w + q) ^ (tok & 7)) << 4));
      const u32 sba = opq(L_SB + tok * 32);
      STAMP(2);
      f32x4 mv = sy.gather_mod(ws, 256 * w + 4 * l, tag);
      if (w >= 2) {
#pragma unroll
        for (int j = 0; j < 4; ++j) mv[j] = rbf(1.f + mv[j]);
      }
      lds_st<f32x4>(L_SB + w * 1024 + l * 16, mv);
      if (w == 0) lds_st<u32x2>(L_CNT + 4 * cTag, u32x2{(u32)tag, (u32)(tag >> 32)});
      ARR(cM2); WAIT(cM2, 4);
      STAMP(3);
      f32x4 sh[4][2], sl[4][2];
#pragma unroll
      for (int i = 0; i < 4; ++i)
#pragma unroll
        for (int e = 0; e < 2; ++e) { sh[i][e] = lds_ld<f32x4>(sba + i * 512 + e * 16); sl[i][e] = lds_ld<f32x4>(sba + 2048 + i * 512 + e * 16); }
      SB();
#pragma unroll
      for (int g = 0; g < 5; ++g) {
#pragma unroll
        for (int i = 0; i < 4; ++i) {
          const u32x4 v = xr[g][i];
          const f32x4 n0 = unp4(v.x, v.y) * rs[g] + nm[g], n1 = unp4(v.z, v.w) * rs[g] + nm[g];
          lds_st<u32x4>(L_H + g * 16384 + ha[i], pack8(n0 * sl[i][0] + sh[i][0], n1 * sl[i][1] + sh[i][1]));
        }
        if (g == 1) { ARR(cH); __builtin_amdgcn_s_setprio(0); STAMP(4); }
      }
      ARR(cH + 1);
      if (w == 0) gflag(ws, l == 0 ? WS_HELLO + wg * 8 : OOB, tag);
      STAMP(5);
    }
    {
      const u32 l = lane_id();
      WAIT(cWc, 4);
      STAMP(6);
      f32x4 mv = sy.gather_mod(ws, 1536 + 256 * w + 4 * l, tag);
      if (w >= 2) {
#pragma unroll
        for (int j = 0; j < 4; ++j) mv[j] = rbf(1.f + mv[j]);
      }
      const srd_t rw = __builtin_amdgcn_make_buffer_rsrc((void*)P.wo, 0, 512 * 1024, 0x00020000);
      const u32 ve = (l >> 3) * 1024 + cg * 128 + ((l & 7) ^ ((l >> 3) & 2)) * 16, vo = (ve ^ 64) + 7168;
      auto issue = [&](int i) {
        const u32 so = (4 * w + ((i + xcd) & 3)) * 32768;
        dma4(rw, L_S + i * 16384 + w * 4096, ve, vo, so, so + 14336);
      };
      if (w == 0) {
        const f32x4 gv = sy.gather_mod(ws, ((l & 16) ? 2560 : 1024) + 64 * cg + 4 * (l & 15), tag);
        lds_st<f32x4>(L_G3 + ((l & 16) ? 512 : 0) + (l & 15) * 16, gv);
      }
      issue(3);
      WAIT(cH + 1, 4);
      const u32 k = (256 * w + 4 * l) & 511;
      lds_st<u32x2>(L_SB + (w < 2 ? 1024 : 0) + ((k - 64 * xcd) & 511) * 2, pack4(mv));
      if (w >= 2 && (k >> 6) == cg) lds_st<f32x4>(L_G3 + 256 + (k & 63) * 4, mv);
      ARR(cM3);
      WAIT(cWc, 8); issue(0);
      WAIT(cWc, 12); issue(1); issue(2);
      STAMP(7);
    }
    u32x4 W[48];
    {
      const u32 l = lane_id(), q = l >> 4, tok = l & 15, jj = (l >> 2) & 3;
      f32x4 uv[3] = {};
      const float ninf = -__builtin_inff();
      const u32 par = tok & 1;
      const bool c0 = 4 * q + par <= tok, c1 = 4 * q + 2 + par <= tok;
      f32x4 bc, bp;
#pragma unroll
      for (int k = 0; k < 4; ++k) {
        const bool pm = (k & 1) == par, cu = k < 2 ? c0 : c1;
        bc[k] = fu(opq(uf(pm && cu ? 0.f : ninf))); bp[k] = fu(opq(uf(pm && !cu ? 0.f : ninf)));
      }
      const u32 s0 = par ? 0x01000c0cu : 0x0c0c0100u, s1 = par ? 0x03020c0cu : 0x0c0c0302u, zz = 0x0c0c0c0cu;
      const u32 sp0 = opq(c0 ? zz : s0), sp1 = opq(c1 ? zz : s1), sc0 = opq(c0 ? s0 : zz), sc1 = opq(c1 ? s1 : zz);
      const u32 dump = opq(L_DUMP + w * 2560 + l * 8), ov = opq((q & 1) * 16384 + (q >> 1) * 256 + tok * 16);
      const u32 fv = opq(l < 8 ? WS_FLAG + ((tq * 8 + l) * 8 + xcd) * 2048 + (cg >> 1) * 128 + (cg & 1) * 8 : OOB);
      u32x2 kp, vp, oa;
      auto attn = [&](int g) {
        WAITP(pQ, g + 1);
        const u32x2 qv = lds_ld<u32x2>(dump), kc = lds_ld<u32x2>(dump + 512), vc = lds_ld<u32x2>(dump + 1024);
        f32x4 b2 = bp;
        if (g == 0) {
          kp = lds_ld<u32x2>(dump + 1536); vp = lds_ld<u32x2>(dump + 2048);
          if (T1 == 0) b2 = f32x4{ninf, ninf, ninf, ninf};
        }
        ARRP(pQf);
        const f32x4 sc = mfma16(kc, qv, bc), sp = mfma16(kp, qv, b2);
        const float e0 = fmaxf(fmaxf(sc[0], sc[1]), fmaxf(sp[0], sp[1])), e1 = fmaxf(fmaxf(sc[2], sc[3]), fmaxf(sp[2], sp[3]));
        const float cs = 0.25f * LOG2E, mx = max32(max16(fmaxf(e0, e1))) * cs;
        const float p0 = __builtin_amdgcn_exp2f(fmaf(e0, cs, -mx)), p1 = __builtin_amdgcn_exp2f(fmaf(e1, cs, -mx));
        const u32 pk = cvtpk(p0, p1);
        const u32x4 pb = {__builtin_amdgcn_perm(0u, pk, sp0), __builtin_amdgcn_perm(0u, pk, sp1), __builtin_amdgcn_perm(0u, pk, sc0), __builtin_amdgcn_perm(0u, pk, sc1)};
        const f32x4 o = mfma32(u32x4{vp.x, vp.y, vc.x, vc.y}, pb, z4);
        const u32x2 ob = pack4(o * __builtin_amdgcn_rcpf(sum32(sum16(p0 + p1))));
        kp = kc; vp = vc;
        if (!(g & 1)) { oa.x = opq(ob.x); oa.y = opq(ob.y); }
        else {
          auto ex = __builtin_amdgcn_permlane16_swap(oa.x, ob.x, false, false);
          auto ey = __builtin_amdgcn_permlane16_swap(oa.y, ob.y, false, false);
          st16_sc1(u32x4{ex[0], ey[0], ex[1], ey[1]}, ws, ov, WS_O + ((T1 >> 4) + g - 1) * 16384 + (4 * cg + w) * 512);
        }
      };
      const srd_t rg = __builtin_amdgcn_make_buffer_rsrc((void*)P.wg, 0, 512 * 2752, 0x00020000);
      const srd_t ru = __builtin_amdgcn_make_buffer_rsrc((void*)P.wu, 0, 512 * 2752, 0x00020000);
      u32 gv[4];
      {
        const u32 row = 8 * w + q, o = fg == 15 ? OOB : (2752 * row + 192 * fg) & ~127u, sl = tok ^ (((row ^ fg) & 1) | (row & 2) | ((row >> 1) & 4));
        gv[0] = opq(o + (sl << 4)); gv[1] = opq(gv[0] + 11008 - 1024); gv[2] = opq(o + ((sl ^ 8) << 4)); gv[3] = opq(gv[2] + 11008 - 1024);
      }
      auto issue = [&](int ks) {
        const u32 so = ((ks + 2 * xcd) & 15) * (32 * 2752), d = L_H + (ks & 3) * 16384 + w * 2048;
        dma2(rg, d, gv[0], gv[1], so); dma2(ru, d + 8192, gv[2], gv[3], so);
      };
      u32 ga[3];
      {
        const u32 m = (l >> 1) & 1, pr = (jj ^ fg) & 1, gx = pr | (jj & 2) | ((q & 1) << 2) | (m << 3);
#pragma unroll
        for (int b = 0; b < 3; ++b) ga[b] = opq(L_H + m * 8192 + (8 * q + jj) * 256 + (((3 * w + b + 4 * pr) ^ gx) << 4) + (l & 1) * 8);
      }
      const u32 sba = opq(L_SB + (l & 1) * 1024 + q * 16);
      auto rd = [&](int ks) {
        const u32x4 sv = lds_ld<u32x4>(sba + ks * 64);
#pragma unroll
        for (int b = 0; b < 3; ++b) W[ks * 3 + b] = tr_frag(ga[b] + (ks & 3) * 16384, 1024);
        SB();
#pragma unroll
        for (int b = 0; b < 3; ++b) uv[b] = mfma32(W[ks * 3 + b], sv, uv[b]);
        SB();
      };
      auto sync = [&](int ks) { ARR(cG); WAIT(cG, 4 * ks + 4); };
      attn(0); STAMP(8);
      WAIT(cA, 4); issue(0); issue(1);
      attn(1); STAMP(9);
      WAIT(cA + 1, 4); issue(2);
      vmwait<13>(); ARRP(pWo);
      WAIT(cM3, 4);
      vmwait<9>(); sync(0); rd(0);
      WAIT(cA + 2, 4); issue(3);
      vmwait<8>(); if (LAST(cPo)) gflag(ws, fv, tag);
      STAMP(10);
      attn(2);
      sync(1); issue(4); rd(1);
      vmwait<8>(); sync(2); issue(5); rd(2);
      attn(3); STAMP(11);
      vmwait<9>(); sync(3); issue(6); rd(3);
      vmwait<9>(); sync(4); issue(7); rd(4);
      vmwait<8>(); if (LAST(cPo + 1)) gflag(ws, fv + 16, tag);
      STAMP(12);
      if (wg == 255 && w == 0) {
#pragma unroll 1
        for (int i = 0; i < 4; ++i) sy.poll(ws, WS_HELLO + i * 512 + l * 8, tag);
        gflag(ws, l == 0 ? WS_EPOCH : OOB, tag);
      }
      sync(5); issue(8); rd(5);
#pragma unroll
      for (int ks = 6; ks < 13; ++ks) { vmwait<8>(); sync(ks); issue(ks + 3); rd(ks); }
      STAMP(13);
      vmwait<8>(); sync(13); rd(13);
      if (w == 0) sy.poll(ws, WS_FLAG + wg * 2048 + 4 * 128 + (lane_id() & 15) * 8, tag);
      vmwait<4>(); sync(14);
      STAMP(14);
      x1_issue(0); x1_issue(1);
      if (w == 0) ARR(cXs);
      rd(14);
      vmwait<16>(); sync(15); rd(15);
      const u32 l2 = lane_id();
      if ((l2 & 15) < 2) {
#pragma unroll
        for (int b = 0; b < 3; ++b) lds_st<f32x4>(L_UV + w * 384 + b * 128 + (l2 & 1) * 64 + (l2 >> 4) * 16, (l2 & 33) == 1 ? uv[b] * -LOG2E : uv[b]);
      }
    }
    {
      const u32 l = lane_id(), q = l >> 4, tok = l & 15;
      u32 xa[4], db = opq(L_GD + w * 6144 + l * 16), cs = sy.cb;
#pragma unroll
      for (int k = 0; k < 4; ++k) xa[k] = opq(L_X0 + tok * 1024 + (((4 * k + q) ^ tok) << 4));
      vmwait<8>(); ARR(cX);
      STAMP(15);
      __builtin_amdgcn_s_setprio(2);
#pragma unroll 1
      for (u32 s = 0; s < 4; ++s) {
        sy.wait<cX>(cs, 4);
        auto xf = [&](int n) { return lds_ld<u32x4>(xa[(n >> 1) & 3] + (n >> 3) * 256 + (n & 1) * 16384); };
        u32x4 a[4];
        f32x4 acc[2][3] = {};
#pragma unroll
        for (int n = 0; n < 4; ++n) a[n] = xf(n);
        SB();
#pragma unroll
        for (int n = 0; n < 32; ++n) {
#pragma unroll
          for (int b = 0; b < 3; ++b) acc[n & 1][b] = mfma32(W[(n >> 1) * 3 + b], a[n & 3], acc[n & 1][b]);
          if (n < 28) a[n & 3] = xf(n + 4);
          SB();
        }
        if (s == 0) { vmwait<0>(); ARR(cX + 1); }
        if (s >= 2) WAITP(pDf, s - 1);
#pragma unroll
        for (int t = 0; t < 2; ++t)
#pragma unroll
          for (int b = 0; b < 3; ++b) lds_st<f32x4>(db + (t * 3 + b) * 1024, acc[t][b]);
        sy.add<cB>(cs);
        cs += 4;
        db += (s & 1) ? -24576 : 24576;
#pragma unroll
        for (int k = 0; k < 4; ++k) xa[k] ^= (L_X0 ^ L_X1);
      }
      __builtin_amdgcn_s_setprio(0);
      STAMP(20);
    }
    {
      const u32 l = lane_id();
      u32 pv = WS_FLAG + wg * 2048 + (8 + 2 * w) * 128 + (l < (w == 1 ? 5 : 4) ? l : 0) * 8;
#pragma unroll 1
      for (u32 s = 0; s < 4; ++s) { sy.poll(ws, pv, tag); ARRP(pF); pv += 64; }
      STAMP(21);
    }
  }
}

extern "C" void kernel_launch(void* const* d_in, const int* in_sizes, int n_in, void* d_out, int out_size, void* d_ws, size_t ws_size,
                              hipStream_t stream) {
  Params p;
  p.x = d_in[0]; p.wqkv = d_in[1]; p.wo = d_in[2]; p.wg = d_in[4]; p.wu = d_in[5]; p.wd = d_in[6]; p.c = d_in[7]; p.wmod = d_in[8];
  p.out = d_out; p.ws = d_ws;
  adaln_block<<<256, 512, 0, stream>>>(p);
}
```

```cpp
#define STAMPS
#include <hip/hip_runtime.h>

#define DI __device__ __forceinline__
#define LAS __attribute__((address_space(3)))
typedef unsigned u32;
typedef unsigned long long u64;
typedef u32 u32x2 __attribute__((ext_vector_type(2)));
typedef u32 u32x4 __attribute__((ext_vector_type(4)));
typedef float f32x2 __attribute__((ext_vector_type(2)));
typedef float f32x4 __attribute__((ext_vector_type(4)));
typedef short s16x4 __attribute__((ext_vector_type(4)));
typedef __bf16 bf16x2 __attribute__((ext_vector_type(2)));
typedef __bf16 bf16x8 __attribute__((ext_vector_type(8)));
typedef __amdgpu_buffer_rsrc_t srd_t;

constexpr u32 WS_EPOCH = 0, WS_HELLO = 4096, WS_MODP = 8192, WS_FLAG = 65536, WS_STAT = 0x100000, WS_O = 0x200000,
              WS_X1 = 0x400000, WS_H2 = 0x600000, WS_STAMP = 0xC00000, WS_END = 0xD00000;
constexpr u32 H2_RB = 172 * 256;
constexpr u32 OOB = 0x80000000u;
constexpr int SC1 = 16;
constexpr u32 L_H = 0, L_S = 81920, L_XR = 147456, L_JUNK = 157440, L_CNT = 157696, L_SB = 157952, L_RS = 162048, L_G3 = 163072;
constexpr u32 L_UV = L_SB + 2048, L_X0 = L_S + 32768, L_X1 = L_H, L_GD = L_H + 32768;
enum { cW = 0, cWc, cM1, cM2, cM3, cG, cR, cXs, cS, cH = 10, cPo = 12, cPx = 14, cA = 16, cB = 20  , cX = 24, cPh = 30,
       pWo = 0, pDf = 4, pF = 8 };
constexpr u32 CODE_BYTES = 32768;
#define SPIN (1 << 16)
#define SLEEPSTR ""
constexpr float LOG2E = 1.4426950408889634f;

constexpr int NSTAMP = 48;
#define CLKI "s_memrealtime"
#define STAMP(k) do { u64 t_; asm volatile(CLKI " %0\n\ts_waitcnt lgkmcnt(0)" : "=s"(t_) :: "memory"); \
    asm volatile("v_writelane_b32 %0, %1, " #k : "+v"(stamps) : "s"((u32)t_)); } while (0)
#define SB() __builtin_amdgcn_sched_barrier(0)

DI u32 rfl(u32 x) { return __builtin_amdgcn_readfirstlane(x); }
DI u32 opq(u32 x) { asm volatile("" : "+v"(x)); return x; }
DI u32 lane_id() { u32 l; asm volatile("v_mbcnt_lo_u32_b32 %0, -1, 0\n\tv_mbcnt_hi_u32_b32 %0, -1, %0" : "=v"(l)); return l; }
DI float fu(u32 x) { return __builtin_bit_cast(float, x); }
DI u32 uf(float x) { return __builtin_bit_cast(u32, x); }
DI float bflo(u32 v) { return fu(v << 16); }
DI float bfhi(u32 v) { return fu(v & 0xffff0000u); }
DI u32 cvtpk(float lo, float hi) { f32x2 v = {lo, hi}; return __builtin_bit_cast(u32, __builtin_convertvector(v, bf16x2)); }
DI u32x2 pack4(f32x4 v) { return u32x2{cvtpk(v[0], v[1]), cvtpk(v[2], v[3])}; }
DI u32x4 pack8(f32x4 a, f32x4 b) { return u32x4{cvtpk(a[0], a[1]), cvtpk(a[2], a[3]), cvtpk(b[0], b[1]), cvtpk(b[2], b[3])}; }
DI f32x4 unp4(u32 a, u32 b) { return f32x4{bflo(a), bfhi(a), bflo(b), bfhi(b)}; }
DI float rbf(float x) { return bflo(cvtpk(x, x)); }
DI float dot2(u32 a, u32 b, float c) { return __builtin_amdgcn_fdot2_f32_bf16(__builtin_bit_cast(bf16x2, a), __builtin_bit_cast(bf16x2, b), c, false); }
DI f32x4 mfma32(u32x4 a, u32x4 b, f32x4 c) {
  return __builtin_amdgcn_mfma_f32_16x16x32_bf16(__builtin_bit_cast(bf16x8, a), __builtin_bit_cast(bf16x8, b), c, 0, 0, 0);
}
DI f32x4 mfma16(u32x2 a, u32x2 b, f32x4 c) {
  return __builtin_amdgcn_mfma_f32_16x16x16bf16_1k(__builtin_bit_cast(s16x4, a), __builtin_bit_cast(s16x4, b), c, 0, 0, 0);
}
template <int C> DI float dpp(float x) { return fu(__builtin_amdgcn_update_dpp(0, (int)uf(x), C, 0xf, 0xf, true)); }
DI float max2(float a, float b) { float r; asm("v_max_f32 %0, %1, %2" : "=v"(r) : "v"(a), "v"(b)); return r; }
DI float sum16(float x) { auto r = __builtin_amdgcn_permlane16_swap(uf(x), uf(x), false, false); return fu(r[0]) + fu(r[1]); }
DI float sum32(float x) { auto r = __builtin_amdgcn_permlane32_swap(uf(x), uf(x), false, false); return fu(r[0]) + fu(r[1]); }
DI float max16(float x) { auto r = __builtin_amdgcn_permlane16_swap(uf(x), uf(x), false, false); return fmaxf(fu(r[0]), fu(r[1])); }
DI float max32(float x) { auto r = __builtin_amdgcn_permlane32_swap(uf(x), uf(x), false, false); return fmaxf(fu(r[0]), fu(r[1])); }

template <class T> DI T lds_ld(u32 a) { return *(LAS T*)a; }
template <class T> DI void lds_st(u32 a, T v) { *(LAS T*)a = v; }
DI u32x4 tr_frag(u32 a, u32 hstep) {
  s16x4 lo = __builtin_amdgcn_ds_read_tr16_b64_v4i16((LAS s16x4*)a);
  s16x4 hi = __builtin_amdgcn_ds_read_tr16_b64_v4i16((LAS s16x4*)(a + hstep));
  u32x2 l = __builtin_bit_cast(u32x2, lo), h = __builtin_bit_cast(u32x2, hi);
  return u32x4{l.x, l.y, h.x, h.y};
}
DI void dma2(srd_t r, u32 lds, u32 v0, u32 v1, u32 so) {
  asm volatile("s_mov_b32 m0, %0\n\ts_nop 0\n\tbuffer_load_dwordx4 %1, %3, %4 offen lds\n\tbuffer_load_dwordx4 %2, %3, %4 offen offset:1024 lds"
               ::"s"(lds), "v"(v0), "v"(v1), "s"(r), "s"(so) : "memory");
}
DI void dma4(srd_t r, u32 lds, u32 v0, u32 v1, u32 s0, u32 s1) {
  asm volatile("s_mov_b32 m0, %0\n\ts_nop 0\n\tbuffer_load_dwordx4 %1, %3, %4 offen lds\n\tbuffer_load_dwordx4 %2, %3, %4 offen offset:1024 lds\n\t"
               "buffer_load_dwordx4 %1, %3, %5 offen offset:2048 lds\n\tbuffer_load_dwordx4 %2, %3, %5 offen offset:3072 lds"
               ::"s"(lds), "v"(v0), "v"(v1), "s"(r), "s"(s0), "s"(s1) : "memory");
}
DI void dma4_sc1(srd_t r, u32 lds, u32 v0, u32 so) {
  asm volatile("s_mov_b32 m0, %0\n\ts_nop 0\n\tbuffer_load_dwordx4 %1, %5, %6 offen sc1 lds\n\tbuffer_load_dwordx4 %2, %5, %6 offen offset:1024 sc1 lds\n\t"
               "buffer_load_dwordx4 %3, %5, %6 offen offset:2048 sc1 lds\n\tbuffer_load_dwordx4 %4, %5, %6 offen offset:3072 sc1 lds"
               ::"s"(lds), "v"(v0), "v"(v0 ^ 16), "v"(v0 ^ 32), "v"(v0 ^ 48), "s"(r), "s"(so) : "memory");
}
DI void touch(srd_t r, u32 voff, u32 soff) {
  asm volatile("s_mov_b32 m0, %0\n\ts_nop 0\n\tbuffer_load_dword %1, %2, %3 offen sc1 lds" ::"s"(L_JUNK), "v"(voff), "s"(r), "s"(soff) : "memory");
}
template <int N> DI void vmwait() { asm volatile("s_waitcnt vmcnt(%0)" ::"i"(N) : "memory"); }
DI void lgwait() { asm volatile("s_waitcnt lgkmcnt(0)" ::: "memory"); }
DI void st16_sc1(u32x4 v, srd_t r, u32 voff, u32 soff) {
  asm volatile("buffer_store_dwordx4 %0, %1, %2, %3 offen sc1\n\ts_nop 1" ::"v"(v), "v"(voff), "s"(r), "s"(soff) : "memory");
}
DI void st16(u32x4 v, srd_t r, u32 voff, u32 soff) {
  asm volatile("buffer_store_dwordx4 %0, %1, %2, %3 offen\n\ts_nop 1" ::"v"(v), "v"(voff), "s"(r), "s"(soff) : "memory");
}
DI void gflag(srd_t r, u32 voff, u64 tag) { __builtin_amdgcn_raw_buffer_store_b64(u32x2{(u32)tag, (u32)(tag >> 32)}, r, voff, 0, SC1); }

struct Sync {
  u32 cb, pb, one; int n;
  template <int I> DI void add(u32 a) {
    asm volatile("s_mov_b64 exec, 1\n\tds_add_u32 %0, %1 offset:%2\n\ts_mov_b64 exec, -1" ::"v"(a), "v"(one), "i"(4 * I) : "memory");
  }
  template <int I> DI u32 add_rtn(u32 a) {
    u32 v, s;
    asm volatile("s_mov_b64 exec, 1\n\tds_add_rtn_u32 %0, %2, %3 offset:%4\n\ts_mov_b64 exec, -1\n\ts_waitcnt lgkmcnt(0)\n\tv_readfirstlane_b32 %1, %0"
                 : "=&v"(v), "=s"(s) : "v"(a), "v"(one), "i"(4 * I) : "memory");
    return s;
  }
  template <int I> DI void wait(u32 a, u32 target) {
    u32 v, s;
    asm volatile("1:\n\tds_read_b32 %0, %3 offset:%5\n\ts_waitcnt lgkmcnt(0)\n\tv_readfirstlane_b32 %1, %0\n\ts_cmp_ge_u32 %1, %4\n\ts_cbranch_scc1 2f\n\t"
                 SLEEPSTR "s_sub_i32 %2, %2, 1\n\ts_cmp_gt_i32 %2, 0\n\ts_cbranch_scc1 1b\n2:"
                 : "=&v"(v), "=&s"(s), "+s"(n) : "v"(a), "s"(target), "i"(4 * I) : "memory", "scc");
  }
  DI void poll(srd_t r, u32 voff, u64 tag) {
    u64 v;
    asm volatile("1:\n\tbuffer_load_dwordx2 %0, %2, %3, 0 offen sc1\n\ts_waitcnt vmcnt(0)\n\tv_cmp_ne_u64 vcc, %4, %0\n\ts_cmp_eq_u64 vcc, 0\n\t"
                 "s_cbranch_scc1 2f\n\ts_sub_i32 %1, %1, 8\n\ts_cmp_gt_i32 %1, 0\n\ts_cbranch_scc1 1b\n2:"
                 : "=&v"(v), "+s"(n) : "v"(voff), "s"(r), "s"(tag) : "memory", "scc", "vcc");
  }
  DI bool poll_once(srd_t r, u32 voff, u64 tag) {
    u64 v, m;
    asm volatile("buffer_load_dwordx2 %0, %2, %3, 0 offen sc1\n\ts_waitcnt vmcnt(0)\n\tv_cmp_ne_u64 %1, %4, %0"
                 : "=&v"(v), "=s"(m) : "v"(voff), "s"(r), "s"(tag) : "memory");
    return m == 0;
  }
  template <bool X> DI f32x4 gather_mod(srd_t ws, u32 col, u64 tag, u32 xo, f32x4& xr) {
    const u32 t0 = tag & 1023, t1 = (tag >> 10) & 1023, t2 = (tag >> 20) & 1023, t3 = (tag >> 30) & 1023;
    u32x4 p[5];
    do {
      asm volatile("" ::: "memory");
      u32 bad = 0;
#pragma unroll
      for (int k = 0; k < 4; ++k) p[k] = __builtin_amdgcn_raw_buffer_load_b128(ws, col * 4, WS_MODP + k * 12288, SC1);
      if (X) p[4] = __builtin_amdgcn_raw_buffer_load_b128(ws, xo, WS_MODP, SC1);
      SB();
#pragma unroll
      for (int k = 0; k < (X ? 5 : 4); ++k) bad |= (p[k].x ^ t0) | (p[k].y ^ t1) | (p[k].z ^ t2) | (p[k].w ^ t3);
      if (__builtin_amdgcn_ballot_w64((bad & 1023) != 0) == 0) break;
      n -= 8;
    } while (n > 0);
    f32x4 r;
#pragma unroll
    for (int j = 0; j < 4; ++j) {
      r[j] = rbf((fu(p[0][j] & ~1023u) + fu(p[1][j] & ~1023u)) + (fu(p[2][j] & ~1023u) + fu(p[3][j] & ~1023u)));
      if (X) xr[j] = rbf(sum32(sum16(fu(p[4][j] & ~1023u))));
    }
    return r;
  }
};
#define ARR(i) sy.add<i>(sy.cb)
#define ARRP(i) sy.add<i>(sy.pb)
#define LAST(i) (sy.add_rtn<i>(sy.cb) == 3)
#define WAIT(i, t) sy.wait<i>(sy.cb, t)
#define WAITP(i, t) sy.wait<i>(sy.pb, t)

struct Params {
  const void *x, *wqkv, *wo, *wg, *wu, *wd, *c, *wmod;
  void *out, *ws;
};

__global__ void __launch_bounds__(512, 2) adaln_block(Params P) {
  __shared__ __attribute__((aligned(16))) char smem[163840];
  const u32 wave = rfl(threadIdx.x >> 6), w = wave & 3;
  const u32 wg = blockIdx.x, xcd = wg & 7, r = wg >> 3, tq = r >> 3, cg = r & 7, fg = r & 15;
  const u32 T1 = xcd * 256 + tq * 64, T3 = xcd * 256 + (r >> 4) * 128;
  asm volatile("" ::"s"((u32)(size_t)(LAS char*)smem) : "memory");
  u32 stamps = 0;
  STAMP(0);
  u64 clk0; asm volatile("s_memtime %0\n\ts_waitcnt lgkmcnt(0)" : "=s"(clk0) :: "memory");
  if (wave == 4) {
    u64 pc;
    asm volatile("s_getpc_b64 %0" : "=s"(pc));
    const u32 l = lane_id();
    touch(__builtin_amdgcn_make_buffer_rsrc((void*)(pc & ~127ull), 0, CODE_BYTES, 0x00020000), (l * 32 + r) * 128, 0);
    lds_st<u32>(L_CNT + 4 * l, 0u);
    lgwait();
  }
  const srd_t ws = __builtin_amdgcn_make_buffer_rsrc(P.ws, 0, WS_END, 0x00020000);
  const srd_t rx = __builtin_amdgcn_make_buffer_rsrc((void*)P.x, 0, 2048 * 1024, 0x00020000);
  const srd_t rqkv = __builtin_amdgcn_make_buffer_rsrc((void*)P.wqkv, 0, 512 * 3072, 0x00020000);
  const f32x4 z4 = {0.f, 0.f, 0.f, 0.f};
  Sync sy;
  sy.cb = opq(L_CNT); sy.pb = opq(L_CNT + 160 + 4 * w); sy.one = opq(1); sy.n = SPIN;
  u64 tag;
  auto x1_issue = [&](int s) {
    const u32 l = lane_id(), xv = fg == 15 ? OOB : (((l ^ (8 * (w & 1))) + 8 * xcd) & 63) * 16;
    const u32 d = ((s & 1) ? L_X1 : L_X0) + w * 8192, so = WS_X1 + (T3 + 64 * (w >> 1) + 16 * s + 8 * (w & 1)) * 1024;
    dma4_sc1(ws, d, xv, so); dma4_sc1(ws, d + 4096, xv ^ 64, so + 4096);
  };
  auto glu = [&](f32x4 g, f32x4 u, u32 uvb, float rs, float mu) {
    const f32x4 ug = lds_ld<f32x4>(uvb), uu = lds_ld<f32x4>(uvb + 32), vg = lds_ld<f32x4>(uvb + 64), vu = lds_ld<f32x4>(uvb + 96);
    const float rn = rs * -LOG2E;
    f32x4 h;
#pragma unroll
    for (int k = 0; k < 4; ++k) {
      const float e = __builtin_amdgcn_exp2f(fmaf(rn, fmaf(-mu, ug[k], g[k]), vg[k]));
      h[k] = fmaf(rs, fmaf(-mu, uu[k], u[k]), vu[k]) * __builtin_amdgcn_rcpf(1.f + e);
    }
    return pack4(h);
  };
  auto publish_h2 = [&](int s, bool last) {
    const u32 l = lane_id();
    if (last) {
      const u32 w1 = (12 * fg) / 44, w2 = (12 * fg + 11) / 44, wc = l < 16 ? w1 : w2, first = wc == 0 ? 0 : 4 * wc - 1;
      const bool on = (l < 16 || (l < 32 && w2 != w1)) && wc < 4;
      gflag(ws, on ? WS_FLAG + (((r & 16) + (l & 15)) * 8 + xcd) * 2048 + (8 + 2 * wc + (s >> 1)) * 128 + (s & 1) * 64 + (fg - first) * 8 : OOB, tag);
    }
    const u32 ln = (12 * fg + 3 * w) * 2 + (l & 7);
    touch(ws, (l < 16 && (l & 7) < 6 && ln < 344) ? (l >> 3) * (4 * H2_RB) + ln * 128 : OOB, WS_H2 + ((T3 >> 4) + s) * H2_RB);
  };

  if (wave < 4) {
    u32x4 W[48];
    f32x4 x1r[2];
    {
      const u32 l = lane_id(), q = l >> 4;
      const srd_t rm = __builtin_amdgcn_make_buffer_rsrc((void*)P.wmod, 0, 512 * 6144, 0x00020000);
      const srd_t rc = __builtin_amdgcn_make_buffer_rsrc((void*)P.c, 0, 1024, 0x00020000);
      const u32 kq = wg / 48, lc = wg - kq * 48, k0 = 128 * kq + 32 * w + 4 * (l >> 3);
      const u32 mv0 = k0 * 6144 + lc * 128 + (l & 7) * 16;
      u32x4 wm[4];
      const u32x2 cv = __builtin_amdgcn_raw_buffer_load_b64(rc, k0 * 2, 0, 0);
#pragma unroll
      for (int i = 0; i < 4; ++i) wm[i] = __builtin_amdgcn_raw_buffer_load_b128(rm, mv0 + i * 6144, 0, 0);
      const u32x2 ep = __builtin_amdgcn_raw_buffer_load_b64(ws, 0, WS_EPOCH, SC1);
      SB();
      __builtin_amdgcn_s_barrier();
      asm volatile("" ::: "memory");
      SB();
      STAMP(32);
      if (kq < 4) {
        float sc[4] = {bflo(cv.x), bfhi(cv.x), bflo(cv.y), bfhi(cv.y)}, a[8] = {};
#pragma unroll
        for (int i = 0; i < 4; ++i) {
          const float s = rbf(sc[i] * __builtin_amdgcn_rcpf(1.f + __builtin_amdgcn_exp2f(sc[i] * -LOG2E)));
#pragma unroll
          for (int j = 0; j < 4; ++j) { a[2 * j] = fmaf(s, bflo(wm[i][j]), a[2 * j]); a[2 * j + 1] = fmaf(s, bfhi(wm[i][j]), a[2 * j + 1]); }
        }
        float b[4], d[2];
#pragma unroll
        for (int j = 0; j < 4; ++j) { auto t = __builtin_amdgcn_permlane32_swap(uf(a[j]), uf(a[j + 4]), false, false); b[j] = fu(t[0]) + fu(t[1]); }
#pragma unroll
        for (int j = 0; j < 2; ++j) { auto t = __builtin_amdgcn_permlane16_swap(uf(b[j]), uf(b[j + 2]), false, false); d[j] = fu(t[0]) + fu(t[1]); }
        d[0] += dpp<0x128>(d[0]); d[1] += dpp<0x128>(d[1]);
        lds_st<f32x2>(L_RS + w * 256 + ((l & 7) * 8 + (l >> 5) * 4 + (q & 1) * 2) * 4, f32x2{d[0], d[1]});
      }
      tag = ((u64)rfl(ep.x) | ((u64)rfl(ep.y) << 32)) + 0x40100401ull;
      asm volatile("" ::"s"(tag));
      STAMP(33);
      if (kq < 4 && LAST(cM1)) {
        const u32 o = L_RS + (l & 15) * 16;
        const f32x4 s = (lds_ld<f32x4>(o) + lds_ld<f32x4>(o + 256)) + (lds_ld<f32x4>(o + 512) + lds_ld<f32x4>(o + 768));
        u32x4 e;
#pragma unroll
        for (int j = 0; j < 4; ++j) e[j] = ((uf(s[j]) + 512u) & ~1023u) | ((u32)(tag >> (10 * j)) & 1023u);
        st16_sc1(e, ws, l < 16 ? l * 16 : OOB, WS_MODP + kq * 12288 + lc * 256);
      }
      STAMP(34);
    }
    {
      const u32 l = lane_id(), q = l >> 4, tok = l & 15, jj = (l >> 2) & 3, drow = l >> 3;
      const u32 v0 = opq((16 * w + drow) * 3072 + cg * 128 + ((l & 7) ^ (drow & 2)) * 16), v1 = opq((v0 ^ 64) + 8 * 3072 - 1024);
      auto issue = [&](int c) {
        const u32 so = ((c + xcd) & 7) * (64 * 3072), d = L_S + (c % 3) * 24576 + w * 2048;
#pragma unroll
        for (int s = 0; s < 3; ++s) dma2(rqkv, d + s * 8192, v0, v1, so + s * 1024);
      };
      issue(0); issue(1); issue(2);
      const u32 tb = opq(L_S + (8 * q + jj) * 128 + (l & 3) * 8 + ((w ^ (jj >> 1) ^ ((q & 1) << 1)) << 5));
      u32 e0 = opq(q * 256 + ((tok ^ q) << 4)), o0 = opq((e0 ^ 64) + 1024);
      auto rd = [&](int c) {
        const u32 t = c % 3 == 2 ? opq(tb + 49152) : tb + (c % 3) * 24576;
#pragma unroll
        for (int kk = 0; kk < 2; ++kk)
#pragma unroll
          for (int s = 0; s < 3; ++s) W[(2 * c + kk) * 3 + s] = tr_frag(t + s * 8192 + kk * 4096, 512);
      };
      auto chunk = [&](int c) {
        if (c == 0) vmwait<12>(); else if (c < 7) vmwait<6>(); else vmwait<0>();
        ARR(cW); WAIT(cW, 4 * c + 4);
        if (c >= 1 && c < 6) issue(c + 2);
        rd(c);
        if (c >= 5) ARR(cWc);
        SB();
      };
      auto hf = [&](int rb, int ks) { return lds_ld<u32x4>(((ks & 1) ? o0 : e0) + rb * 16384 + (ks >> 1) * 2048); };
      chunk(0); STAMP(1); chunk(1); chunk(2); chunk(3);
      STAMP(2);
      WAIT(cH, 4);
      __builtin_amdgcn_s_setprio(2);
      STAMP(3);
      u32x4 a[4];
      u32x2 qb[2], kb[3], vb[3];
      {
        f32x4 aq[2] = {}, ak[3] = {}, av[3] = {};
#pragma unroll
        for (int n = 0; n < 4; ++n) a[n] = hf(n % 3, n / 3);
        SB();
#pragma unroll
        for (int n = 0; n < 48; ++n) {
          const int ks = n / 3, rb = n % 3;
          if (n == 12) chunk(4);
          if (n == 24) chunk(5);
          if (n == 36) chunk(6);
          if (n == 42) { chunk(7); STAMP(4); }
          if (rb) aq[rb - 1] = mfma32(W[ks * 3], a[n & 3], aq[rb - 1]);
          ak[rb] = mfma32(W[ks * 3 + 1], a[n & 3], ak[rb]); av[rb] = mfma32(a[n & 3], W[ks * 3 + 2], av[rb]);
          if (n + 4 < 48) a[n & 3] = hf((n + 4) % 3, (n + 4) / 3);
          SB();
        }
        ARR(cA);
#pragma unroll
        for (int k = 0; k < 3; ++k) { kb[k] = pack4(ak[k]); vb[k] = pack4(av[k]); }
        qb[0] = pack4(aq[0]); qb[1] = pack4(aq[1]);
      }
      auto attn = [&](int j) {
        const u32 l = lane_id(), q = l >> 4, tok = l & 15, par = tok & 1;
        const float ninf = -__builtin_inff();
        const bool c0 = 4 * q + par <= tok, c1 = 4 * q + 2 + par <= tok;
        f32x4 bc, bp;
#pragma unroll
        for (int k = 0; k < 4; ++k) { const bool pm = (k & 1) == par, cu = k < 2 ? c0 : c1; bc[k] = pm && cu ? 0.f : ninf; bp[k] = pm && !cu ? 0.f : ninf; }
        const u32 s0 = par ? 0x01000c0cu : 0x0c0c0100u, s1 = par ? 0x03020c0cu : 0x0c0c0302u, zz = 0x0c0c0c0cu;
        const u32 sp0 = c0 ? zz : s0, sp1 = c1 ? zz : s1, sc0 = c0 ? s0 : zz, sc1 = c1 ? s1 : zz;
        u32x2 ob[2];
#pragma unroll
        for (int t = 0; t < 2; ++t) {
          f32x4 b2 = bp;
          if (j == 0 && t == 0 && T1 == 0) b2 = f32x4{ninf, ninf, ninf, ninf};
          const f32x4 sc = mfma16(kb[t + 1], qb[t], bc), sp = mfma16(kb[t], qb[t], b2);
          const float e0 = fmaxf(fmaxf(sc[0], sc[1]), fmaxf(sp[0], sp[1])), e1 = fmaxf(fmaxf(sc[2], sc[3]), fmaxf(sp[2], sp[3]));
          const float cs = 0.25f * LOG2E, mx = max32(max16(fmaxf(e0, e1))) * cs;
          const float p0 = __builtin_amdgcn_exp2f(fmaf(e0, cs, -mx)), p1 = __builtin_amdgcn_exp2f(fmaf(e1, cs, -mx));
          const u32 pk = cvtpk(p0, p1);
          const u32x4 pb = {__builtin_amdgcn_perm(0u, pk, sp0), __builtin_amdgcn_perm(0u, pk, sp1), __builtin_amdgcn_perm(0u, pk, sc0), __builtin_amdgcn_perm(0u, pk, sc1)};
          const f32x4 o = mfma32(u32x4{vb[t].x, vb[t].y, vb[t + 1].x, vb[t + 1].y}, pb, z4);
          ob[t] = pack4(o * __builtin_amdgcn_rcpf(sum32(sum16(p0 + p1))));
        }
        auto ex = __builtin_amdgcn_permlane16_swap(ob[0].x, ob[1].x, false, false);
        auto ey = __builtin_amdgcn_permlane16_swap(ob[0].y, ob[1].y, false, false);
        st16_sc1(u32x4{ex[0], ey[0], ex[1], ey[1]}, ws, (q & 1) * 16384 + (q >> 1) * 256 + tok * 16, WS_O + ((T1 >> 4) + 2 * j) * 16384 + (4 * cg + w) * 512);
      };
      auto publish_o = [&](int j, bool last) {
        const u32 l = lane_id();
        if (last) gflag(ws, l < 8 ? WS_FLAG + ((tq * 8 + l) * 8 + xcd) * 2048 + (cg >> 1) * 128 + (cg & 1) * 8 + j * 16 : OOB, tag);
        touch(ws, l < 8 ? (l >> 2) * 16384 + (l & 3) * 128 : OOB, WS_O + ((T1 >> 4) + 2 * j) * 16384 + (4 * cg + w) * 512);
      };
      STAMP(5);
      attn(0);
      STAMP(6);
      WAIT(cH + 1, 4);
      {
        e0 += 49152; o0 += 49152;
        f32x4 aq[2] = {}, ak[2] = {}, av[2] = {};
#pragma unroll
        for (int n = 0; n < 4; ++n) a[n] = hf(n & 1, n >> 1);
        SB();
#pragma unroll
        for (int n = 0; n < 32; ++n) {
          const int ks = n >> 1, rb = n & 1;
          if (n == 16) { vmwait<0>(); publish_o(0, LAST(cPo)); STAMP(27); SB(); }
          aq[rb] = mfma32(W[ks * 3], a[n & 3], aq[rb]); ak[rb] = mfma32(W[ks * 3 + 1], a[n & 3], ak[rb]); av[rb] = mfma32(a[n & 3], W[ks * 3 + 2], av[rb]);
          if (n + 4 < 32) a[n & 3] = hf(n & 1, (n + 4) >> 1);
          SB();
        }
        ARR(cA + 1);
        kb[0] = kb[2]; vb[0] = vb[2];
#pragma unroll
        for (int k = 0; k < 2; ++k) { kb[k + 1] = pack4(ak[k]); vb[k + 1] = pack4(av[k]); qb[k] = pack4(aq[k]); }
      }
      STAMP(35);
      attn(1);
      __builtin_amdgcn_s_setprio(0);
      STAMP(36);
      const u32 l2 = lane_id();
      {
      const u32 l = l2, q = l >> 4, tok = l & 15, jj = (l >> 2) & 3;
      const u32 c8 = l & 7, rtk = 8 * (w & 1) + (l >> 3);
      const u32 ev = opq((8 * w + (l >> 3)) * 1024 + c8 * 16);
      u32x4 xres[2];
#pragma unroll
      for (int j = 0; j < 2; ++j) xres[j] = __builtin_amdgcn_raw_buffer_load_b128(rx, ev, (T1 + 32 * j) * 1024 + cg * 128, 0);
      SB();
      const u32 tb = L_S + w * 4096 + (8 * q + jj) * 128 + (l & 3) * 8 + (((jj >> 1) ^ ((q & 1) << 1)) << 5);
      WAITP(pWo, 1);
#pragma unroll
      for (int cb = 0; cb < 4; ++cb) {
        const u32 b = opq(tb ^ (cb << 5));
#pragma unroll
        for (int i = 0; i < 4; ++i) W[i * 4 + cb] = tr_frag(b + i * 16384, 512);
      }
      ARR(cR);
      const u32 pv = opq(WS_FLAG + wg * 2048 + w * 128 + (l & 1) * 8);
      bool ok = sy.poll_once(ws, pv, tag);
      publish_o(1, LAST(cPo + 1));
      const u32 wa = opq(L_S + w * 4096 + tok * 256 + ((q ^ (tok & 7)) << 4)), wb = opq(wa ^ 64);
      const u32 r0 = opq(L_S + (w >> 1) * 16384 + rtk * 256 + (((2 * c8) ^ (rtk & 7)) << 4)), r1 = opq(r0 ^ 16);
      const u32 l16 = opq(l * 16), sv = opq(c8 == 0 ? rtk * 8 : OOB);
      const u32 fv = opq(l < 16 ? WS_FLAG + (((r & 16) + l) * 8 + xcd) * 2048 + 4 * 128 + fg * 8 : OOB);
      STAMP(7);
#pragma unroll
      for (int j = 0; j < 2; ++j) {
        if (!ok) sy.poll(ws, pv + j * 16, tag);
        if (j == 0) STAMP(8); else STAMP(12);
        u32x4 a[2][4];
#pragma unroll
        for (int i = 0; i < 4; ++i)
#pragma unroll
          for (int rb = 0; rb < 2; ++rb)
            a[rb][i] = __builtin_amdgcn_raw_buffer_load_b128(ws, l16, WS_O + ((T1 >> 4) + 2 * j + rb) * 16384 + (4 * w + ((i + xcd) & 3)) * 1024, SC1);
        SB();
        if (j == 0) WAIT(cM3, 4);
        WAIT(cR, 4 + 8 * j);
        const u32 g3 = L_G3 + c8 * 32;
        const f32x4 ga0 = lds_ld<f32x4>(g3), ga1 = lds_ld<f32x4>(g3 + 16), s0 = lds_ld<f32x4>(g3 + 256), s1 = lds_ld<f32x4>(g3 + 272);
        SB();
        f32x4 acc[2][4] = {};
#pragma unroll
        for (int i = 0; i < 4; ++i)
#pragma unroll
          for (int rb = 0; rb < 2; ++rb) {
#pragma unroll
            for (int cb = 0; cb < 4; ++cb) acc[rb][cb] = mfma32(W[i * 4 + cb], a[rb][i], acc[rb][cb]);
            SB();
          }
#pragma unroll
        for (int rb = 0; rb < 2; ++rb) {
          lds_st<f32x4>(wa + rb * 16384, acc[rb][0]); lds_st<f32x4>(wb + rb * 16384, acc[rb][1]);
          lds_st<f32x4>(wa + rb * 16384 + 128, acc[rb][2]); lds_st<f32x4>(wb + rb * 16384 + 128, acc[rb][3]);
        }
        ARR(cR);
        if (j == 0) STAMP(9); else STAMP(13);
        WAIT(cR, 8 + 8 * j);
        const f32x4 y0 = (lds_ld<f32x4>(r0) + lds_ld<f32x4>(r0 + 4096)) + (lds_ld<f32x4>(r0 + 8192) + lds_ld<f32x4>(r0 + 12288));
        const f32x4 y1 = (lds_ld<f32x4>(r1) + lds_ld<f32x4>(r1 + 4096)) + (lds_ld<f32x4>(r1 + 8192) + lds_ld<f32x4>(r1 + 12288));
        ARR(cR);
        const f32x4 u0 = unp4(xres[j].x, xres[j].y) + ga0 * y0, u1 = unp4(xres[j].z, xres[j].w) + ga1 * y1;
        st16_sc1(pack8(u0 * s0, u1 * s1), ws, ev, WS_X1 + (T1 + 32 * j) * 1024 + cg * 128);
        const f32x4 t1 = u0 + u1, t2 = u0 * u0 + u1 * u1;
        float sm = (t1[0] + t1[1]) + (t1[2] + t1[3]), sq = (t2[0] + t2[1]) + (t2[2] + t2[3]);
        sm += dpp<0xB1>(sm); sq += dpp<0xB1>(sq);
        sm += dpp<0x4E>(sm); sq += dpp<0x4E>(sq);
        sm += dpp<0x141>(sm); sq += dpp<0x141>(sq);
        __builtin_amdgcn_raw_buffer_store_b64(u32x2{uf(sm), uf(sq)}, ws, sv, WS_STAT + (((T1 >> 4) + 2 * j + (w >> 1)) * 8 + cg) * 128, SC1);
        if (j == 0) { lds_st<f32x4>(L_XR + w * 2048 + l16, u0); lds_st<f32x4>(L_XR + w * 2048 + 1024 + l16, u1); }
        else { x1r[0] = u0; x1r[1] = u1; }
        if (j == 0) STAMP(10); else STAMP(14);
        if (j == 0) ok = sy.poll_once(ws, pv + 16, tag); else vmwait<0>();
        if (j == 0 ? LAST(cPx) : LAST(cPx + 1)) gflag(ws, fv + j * 128, tag);
        touch(ws, c8 == 0 ? ev : OOB, WS_X1 + (T1 + 32 * j) * 1024 + cg * 128);
        if (j == 0) STAMP(11); else STAMP(15);
      }
      }
    }
    {
      const srd_t rd = __builtin_amdgcn_make_buffer_rsrc((void*)P.wd, 0, 1376 * 1024, 0x00020000);
      auto kstep = [&](int i) { const u32 a = i + xcd, b = a - 11; return 11 * w + (a < b ? a : b); };
      auto wd_issue = [&](int rnd) {
        const u32 l = lane_id(), v0 = (l >> 3) * 1024 + cg * 128 + ((l & 7) ^ ((l >> 3) & 2)) * 16;
#pragma unroll
        for (int i = 2 * rnd; i < 2 * rnd + 2 && i < 11; ++i) {
          const u32 ks = kstep(i), ve = ks > 42 ? OOB : v0, so = ks > 42 ? 0 : ks * 32768;
          dma4(rd, L_S + w * 8192 + (i & 1) * 4096, ve, (ve ^ 64) + 7168, so, so + 14336);
        }
      };
      auto wd_read = [&](int rnd) {
        const u32 l = lane_id(), q = l >> 4, jj = (l >> 2) & 3;
        const u32 b0 = L_S + w * 8192 + (8 * q + jj) * 128 + (l & 3) * 8 + (((jj >> 1) ^ ((q & 1) << 1)) << 5);
#pragma unroll
        for (int cb = 0; cb < 4; ++cb) {
          const u32 b = opq(b0 ^ (cb << 5));
#pragma unroll
          for (int i = 2 * rnd; i < 2 * rnd + 2 && i < 11; ++i) W[i * 4 + cb] = tr_frag(b + (i & 1) * 4096, 512);
        }
        lgwait();
      };
      auto stats_ld = [&](int j) {
        u32x4 p;
        asm volatile("buffer_load_dwordx4 %0, %1, %2, %3 offen sc1" : "=v"(p)
                     : "v"(lane_id() * 16), "s"(ws), "s"(WS_STAT + ((T3 >> 4) + 4 * (w >> 1) + 2 * j + (w & 1)) * 1024) : "memory");
        return p;
      };
      auto stats = [&](int j, u32x4 p) {
        asm volatile("" : "+v"(p));
        float v[4];
#pragma unroll
        for (int k = 0; k < 4; ++k) { float t = fu(p[k]); t += dpp<0x128>(t); v[k] = sum32(sum16(t)); }
        const float m0 = v[0] * (1.f / 512), m1 = v[2] * (1.f / 512);
        const float r0 = __builtin_amdgcn_rsqf(fmaxf(v[1] * (1.f / 512) - m0 * m0, 0.f) + 1e-5f);
        const float r1 = __builtin_amdgcn_rsqf(fmaxf(v[3] * (1.f / 512) - m1 * m1, 0.f) + 1e-5f);
        lds_st<f32x4>(L_RS + ((2 * j + (w & 1)) * 2 + (w >> 1)) * 128 + (lane_id() & 7) * 16, f32x4{r0, m0, r1, m1});
        if (j == 0) ARR(cS); else ARR(cS + 1);
      };
      u32 d01, d2, uv01, uv2, sta, hv0, hv1;
      {
        const u32 l = lane_id(), ti = l >> 5, bq = (l >> 4) & 1, tok = l & 15, db = L_GD + w * 6144 + ti * 3072 + tok * 16;
        const u32 pc = 12 * fg + 3 * w, vb = ti * (4 * H2_RB) + tok * 16;
        d01 = opq(db + bq * 1024); d2 = opq(db + 2048 + bq * 256); uv01 = opq(L_UV + w * 384 + bq * 128); uv2 = opq(L_UV + w * 384 + 256 + bq * 16);
        sta = opq(L_RS + ti * 128 + tok * 8);
        hv0 = opq(pc + bq < 172 ? vb + (pc + bq) * 256 : OOB); hv1 = opq((bq == 0 && pc + 2 < 172) ? vb + (pc + 2) * 256 : OOB);
      }
      auto epilogue = [&](int s) {
        const u32 o = (s & 1) * 24576, so = WS_H2 + ((T3 >> 4) + s) * H2_RB;
        const f32x2 st = lds_ld<f32x2>(sta + s * 256);
        const f32x4 g0 = lds_ld<f32x4>(d01 + o), u0 = lds_ld<f32x4>(d01 + o + 512), g1 = lds_ld<f32x4>(d01 + o + 256), u1 = lds_ld<f32x4>(d01 + o + 768);
        const u32x2 h0 = glu(g0, u0, uv01, st[0], st[1]);
        SB();
        const f32x4 g2 = lds_ld<f32x4>(d2 + o), u2 = lds_ld<f32x4>(d2 + o + 512);
        const u32x2 h1 = glu(g1, u1, uv01 + 16, st[0], st[1]);
        st16_sc1(u32x4{h0.x, h0.y, h1.x, h1.y}, ws, hv0, so);
        ARRP(pDf);
        const u32x2 h2 = glu(g2, u2, uv2, st[0], st[1]);
        auto ex = __builtin_amdgcn_permlane16_swap(h2.x, h2.x, false, false);
        auto ey = __builtin_amdgcn_permlane16_swap(h2.y, h2.y, false, false);
        st16_sc1(u32x4{ex[0], ey[0], ex[1], ey[1]}, ws, hv1, so);
      };
      WAIT(cR, 20);
      WAIT(cXs, 1);
      STAMP(16);
      u32x4 sp = stats_ld(0);
      wd_issue(0);
      vmwait<8>(); stats(0, sp);
      vmwait<0>(); wd_read(0); wd_issue(1);
      if (w == 0) { sy.poll(ws, WS_FLAG + wg * 2048 + 5 * 128 + (lane_id() & 15) * 8, tag); ARR(cXs); }
      vmwait<0>(); wd_read(1);
      WAIT(cXs, 2);
      sp = stats_ld(1);
      wd_issue(2);
      WAIT(cS, 4);
      STAMP(17);
      WAIT(cB, 4); STAMP(18); x1_issue(2); epilogue(0);
      vmwait<2>(); ARR(cX + 2);
      wd_read(2); wd_issue(3);
      stats(1, sp);
      vmwait<8>(); publish_h2(0, LAST(cPh));
      STAMP(19);
      WAIT(cB + 1, 4); STAMP(20); x1_issue(3); epilogue(1);
      vmwait<2>(); ARR(cX + 3);
      wd_read(3); wd_issue(4);
      vmwait<8>(); publish_h2(1, LAST(cPh + 1));
      STAMP(21);
      vmwait<0>(); wd_read(4); wd_issue(5);
      WAIT(cS + 1, 4);
      WAIT(cB + 2, 4); STAMP(22); epilogue(2);
      vmwait<2>(); wd_read(5);
      STAMP(23);
      const srd_t ro = __builtin_amdgcn_make_buffer_rsrc(P.out, 0, 2048 * 1024, 0x00020000);
      const u32 l = lane_id(), l16 = opq(l * 16);
      {
        const u32 pv = WS_FLAG + wg * 2048 + (8 + 2 * w) * 128 + (l & 8) * 8 + ((l & 7) < (w == 1 ? 5 : 4) ? (l & 7) : 0) * 8;
        const bool ok = sy.poll_once(ws, pv, tag);
        publish_h2(2, LAST(cPh + 2));
        if (!ok) sy.poll(ws, pv, tag);
      }
      STAMP(24);
      u32x4 a[8];
      auto h2_ld = [&](int n) {
        const u32 ks = kstep(n % 11);
        a[n & 7] = __builtin_amdgcn_raw_buffer_load_b128(ws, l16, WS_H2 + ((T1 >> 4) + n / 11) * H2_RB + (ks > 42 ? 42 : ks) * 1024, SC1);
      };
#pragma unroll
      for (int n = 0; n < 8; ++n) h2_ld(n);
      SB();
      const u32 c8 = l & 7, rtk = 8 * (w & 1) + (l >> 3);
      const u32 wa = opq(L_X0 + w * 4096 + (l & 15) * 256 + (((l >> 4) ^ (l & 7)) << 4));
      const u32 r0 = opq(L_X0 + (w >> 1) * 16384 + rtk * 256 + (((2 * c8) ^ (rtk & 7)) << 4));
#pragma unroll
      for (int g = 0; g < 4; ++g) {
        f32x4 acc[4] = {};
        SB();
#pragma unroll
        for (int i = 0; i < 11; ++i) {
          const int n = 11 * g + i;
          if (n == 14) { WAITP(pF, 1); SB(); }
#pragma unroll
          for (int cb = 0; cb < 4; ++cb) acc[cb] = mfma32(W[i * 4 + cb], a[n & 7], acc[cb]);
          if (n + 8 < 33 || (g == 3 && n + 8 < 44)) h2_ld(n + 8);
          SB();
        }
        {
          const u32 wb = wa ^ 64, o = (g & 1) * 16384;
          lds_st<f32x4>(wa + o, acc[0]); lds_st<f32x4>(wb + o, acc[1]); lds_st<f32x4>(wa + o + 128, acc[2]); lds_st<f32x4>(wb + o + 128, acc[3]);
        }
        ARR(cR);
        if (g == 0) STAMP(25);
        if (g == 1) STAMP(26);
        if (g == 3) STAMP(30);
        if (g == 2) {
          STAMP(28);
          WAITP(pF, 2);
          STAMP(29);
#pragma unroll
          for (int n = 33; n < 41; ++n) h2_ld(n);
          SB();
        }
        WAIT(cR, 24 + 4 * g);
        if ((w >> 1) == (g & 1)) {
          const u32 r1 = r0 ^ 16, gb = opq(L_G3 + 512 + (l16 & 112) * 2), xa = opq(L_XR + w * 2048 + l16);
          const f32x4 y0 = (lds_ld<f32x4>(r0) + lds_ld<f32x4>(r0 + 4096)) + (lds_ld<f32x4>(r0 + 8192) + lds_ld<f32x4>(r0 + 12288));
          const f32x4 o0 = (g < 2 ? lds_ld<f32x4>(xa) : x1r[0]) + lds_ld<f32x4>(gb) * y0;
          if (g < 3) SB();
          const f32x4 y1 = (lds_ld<f32x4>(r1) + lds_ld<f32x4>(r1 + 4096)) + (lds_ld<f32x4>(r1 + 8192) + lds_ld<f32x4>(r1 + 12288));
          const f32x4 o1 = (g < 2 ? lds_ld<f32x4>(xa + 1024) : x1r[1]) + lds_ld<f32x4>(gb + 16) * y1;
          st16(pack8(o0, o1), ro, (l16 >> 7) * 1024 + (l16 & 112), (T1 + 32 * (g >> 1) + 8 * w) * 1024 + cg * 128);
        }
      }
      STAMP(31);
    }
  } else {
    const srd_t rwo = __builtin_amdgcn_make_buffer_rsrc((void*)P.wo, 0, 512 * 1024, 0x00020000);
    {
      const u32 l = lane_id(), q = l >> 4, tok = l & 15;
      const u32 xo = (T1 - 16 + 4 * w + q) * 1024 + tok * 16;
      const u32 tx = opq(l < 20 ? (T1 - 16) * 1024 + (80 * cg + 20 * w + l) * 128 : OOB);
      const u32 tw = opq(l < 12 ? (48 * r + 12 * w + l) * 1024 + ((3 - ((u32)(size_t)P.wqkv >> 7)) & 7) * 128 : OOB);
      const u32 to = opq(l < 4 ? (16 * r + 4 * w + l) * 1024 + ((3 - ((u32)(size_t)P.wo >> 7)) & 7) * 128 : OOB);
      const u32 tf = opq(l < 4 ? (4 * w + l) * 128 : OOB);
      __builtin_amdgcn_s_barrier();
      asm volatile("" ::: "memory");
      touch(rx, tx, 0); touch(rqkv, tw, 0); touch(rwo, to, 0); touch(ws, tf, WS_FLAG + wg * 2048);
      const u32x2 ep = __builtin_amdgcn_raw_buffer_load_b64(ws, 0, WS_EPOCH, SC1);
      u32x4 xr[5][4];
#pragma unroll
      for (int g = 0; g < 5; ++g) {
        const u32 o = opq(xo + g * 16384);
#pragma unroll
        for (int i = 0; i < 4; ++i) xr[g][i] = __builtin_amdgcn_raw_buffer_load_b128(rx, o + i * 256, 0, 0);
      }
      SB();
      STAMP(1);
      tag = ((u64)rfl(ep.x) | ((u64)rfl(ep.y) << 32)) + 0x40100401ull;
      f32x4 xn[5][4][2];
#pragma unroll
      for (int g = 0; g < 5; ++g) {
        float s = 0.f, ss = 0.f;
#pragma unroll
        for (int i = 0; i < 4; ++i)
#pragma unroll
          for (int j = 0; j < 4; ++j) { s = dot2(xr[g][i][j], 0x3f803f80u, s); ss = dot2(xr[g][i][j], xr[g][i][j], ss); }
        s += dpp<0x128>(s); ss += dpp<0x128>(ss);
        s += dpp<0x124>(s); ss += dpp<0x124>(ss);
        s += dpp<0x122>(s); ss += dpp<0x122>(ss);
        s += dpp<0x121>(s); ss += dpp<0x121>(ss);
        const float m = s * (1.f / 512), rs = __builtin_amdgcn_rsqf(fmaxf(ss * (1.f / 512) - m * m, 0.f) + 1e-5f), nm = -m * rs;
#pragma unroll
        for (int i = 0; i < 4; ++i) {
          xn[g][i][0] = unp4(xr[g][i].x, xr[g][i].y) * rs + nm; xn[g][i][1] = unp4(xr[g][i].z, xr[g][i].w) * rs + nm;
          if (g < 3) asm volatile("" : "+v"(xn[g][i][0]), "+v"(xn[g][i][1]));
        }
        if (g == 2) STAMP(2);
        if (g == 2) break;
      }
      u32 ha[4];
#pragma unroll
      for (int i = 0; i < 4; ++i) ha[i] = opq(((16 * i + tok - 8 * xcd) & 63) * 256 + (((4 * w + q) ^ (tok & 7)) << 4));
      const u32 sba = opq(L_SB + tok * 32);
      __builtin_amdgcn_s_setprio(3);
      f32x4 dummy, mv = sy.gather_mod<false>(ws, 256 * w + 4 * l, tag, 0, dummy);
      if (w >= 2) {
#pragma unroll
        for (int j = 0; j < 4; ++j) mv[j] = rbf(1.f + mv[j]);
      }
      lds_st<f32x4>(L_SB + w * 1024 + l * 16, mv);
      ARR(cM2); WAIT(cM2, 4);
      STAMP(3);
      f32x4 sh[4][2], sl[4][2];
#pragma unroll
      for (int i = 0; i < 4; ++i)
#pragma unroll
        for (int e = 0; e < 2; ++e) { sh[i][e] = lds_ld<f32x4>(sba + i * 512 + e * 16); sl[i][e] = lds_ld<f32x4>(sba + 2048 + i * 512 + e * 16); }
      SB();
#pragma unroll
      for (int i = 0; i < 4; ++i)
#pragma unroll
        for (int g = 0; g < 3; ++g) lds_st<u32x4>(L_H + g * 16384 + ha[i], pack8(xn[g][i][0] * sl[i][0] + sh[i][0], xn[g][i][1] * sl[i][1] + sh[i][1]));
      ARR(cH);
      __builtin_amdgcn_s_setprio(0);
      STAMP(4);
#pragma unroll
      for (int g = 3; g < 5; ++g) {
        float s = 0.f, ss = 0.f;
#pragma unroll
        for (int i = 0; i < 4; ++i)
#pragma unroll
          for (int j = 0; j < 4; ++j) { s = dot2(xr[g][i][j], 0x3f803f80u, s); ss = dot2(xr[g][i][j], xr[g][i][j], ss); }
        s += dpp<0x128>(s); ss += dpp<0x128>(ss);
        s += dpp<0x124>(s); ss += dpp<0x124>(ss);
        s += dpp<0x122>(s); ss += dpp<0x122>(ss);
        s += dpp<0x121>(s); ss += dpp<0x121>(ss);
        const float m = s * (1.f / 512), rs = __builtin_amdgcn_rsqf(fmaxf(ss * (1.f / 512) - m * m, 0.f) + 1e-5f), nm = -m * rs;
#pragma unroll
        for (int i = 0; i < 4; ++i) {
          const f32x4 n0 = unp4(xr[g][i].x, xr[g][i].y) * rs + nm, n1 = unp4(xr[g][i].z, xr[g][i].w) * rs + nm;
          lds_st<u32x4>(L_H + 49152 + (g - 3) * 16384 + ha[i], pack8(n0 * sl[i][0] + sh[i][0], n1 * sl[i][1] + sh[i][1]));
        }
      }
      ARR(cH + 1);
      STAMP(5);
    }
    u32x4 W[48];
    {
      const u32 l = lane_id(), q = l >> 4, tok = l & 15, jj = (l >> 2) & 3;
      const u32 ve = opq((l >> 3) * 1024 + cg * 128 + ((l & 7) ^ ((l >> 3) & 2)) * 16), vo = opq((ve ^ 64) + 7168);
      auto wo_issue = [&](int i) {
        const u32 so = (4 * w + ((i + xcd) & 3)) * 32768;
        dma4(rwo, L_S + i * 16384 + w * 4096, ve, vo, so, so + 14336);
      };
      WAIT(cWc, 4);
      STAMP(6);
      wo_issue(3);
      if (w == 0) gflag(ws, l == 0 ? WS_HELLO + wg * 8 : OOB, tag);
      f32x4 gv4, mv = sy.gather_mod<true>(ws, 1536 + 256 * w + 4 * l, tag, q * 12288 + (((w & 1) ? 2560 : 1024) + 64 * cg + 4 * tok) * 4, gv4);
      WAIT(cWc, 8); wo_issue(0);
      if (w >= 2) {
#pragma unroll
        for (int j = 0; j < 4; ++j) mv[j] = rbf(1.f + mv[j]);
      }
      lds_st<f32x4>(L_G3 + (w & 1) * 512 + tok * 16, gv4);
      WAIT(cH + 1, 4);
      const u32 k = (256 * w + 4 * l) & 511;
      lds_st<u32x2>(L_SB + (w < 2 ? 1024 : 0) + ((k - 64 * xcd) & 511) * 2, pack4(mv));
      if (w >= 2 && (k >> 6) == cg) lds_st<f32x4>(L_G3 + 256 + (k & 63) * 4, mv);
      ARR(cM3);
      WAIT(cWc, 12); wo_issue(1); wo_issue(2);
      STAMP(7);
      f32x4 uv[3] = {};
      const srd_t rg = __builtin_amdgcn_make_buffer_rsrc((void*)P.wg, 0, 512 * 2752, 0x00020000);
      const srd_t ru = __builtin_amdgcn_make_buffer_rsrc((void*)P.wu, 0, 512 * 2752, 0x00020000);
      u32 gv[4];
      {
        const u32 row = 8 * w + q, o = fg == 15 ? OOB : (2752 * row + 192 * fg) & ~127u, sl = tok ^ (((row ^ fg) & 1) | (row & 2) | ((row >> 1) & 4));
        gv[0] = opq(o + (sl << 4)); gv[1] = opq(gv[0] + 11008 - 1024); gv[2] = opq(o + ((sl ^ 8) << 4)); gv[3] = opq(gv[2] + 11008 - 1024);
      }
      constexpr int slot[16] = {0, 1, 2, 0, 1, 3, 2, 0, 1, 3, 2, 0, 1, 3, 2, 4};
      auto issue = [&](int ks) {
        const u32 so = ((ks + 2 * xcd) & 15) * (32 * 2752), d = L_H + slot[ks] * 16384 + w * 2048;
        dma2(rg, d, gv[0], gv[1], so); dma2(ru, d + 8192, gv[2], gv[3], so);
      };
      u32 ga[3];
      {
        const u32 m = (l >> 1) & 1, pr = (jj ^ fg) & 1, gx = pr | (jj & 2) | ((q & 1) << 2) | (m << 3);
#pragma unroll
        for (int b = 0; b < 3; ++b) ga[b] = opq(L_H + m * 8192 + (8 * q + jj) * 256 + (((3 * w + b + 4 * pr) ^ gx) << 4) + (l & 1) * 8);
      }
      const u32 sba = opq(L_SB + (l & 1) * 1024 + q * 16);
      auto rd = [&](int ks) {
        const u32x4 sv = lds_ld<u32x4>(sba + ks * 64);
#pragma unroll
        for (int b = 0; b < 3; ++b) W[ks * 3 + b] = tr_frag(ga[b] + slot[ks] * 16384, 1024);
        SB();
#pragma unroll
        for (int b = 0; b < 3; ++b) uv[b] = mfma32(W[ks * 3 + b], sv, uv[b]);
        SB();
      };
      auto sync = [&](int ks) { ARR(cG); WAIT(cG, 4 * ks + 4); };
      WAIT(cA, 4); issue(0); issue(1); issue(2);
      STAMP(8);
      vmwait<12>(); ARRP(pWo);
      WAIT(cM3, 4);
      vmwait<8>(); sync(0); rd(0);
      STAMP(9);
      vmwait<4>(); sync(1); issue(3); rd(1);
      vmwait<4>(); sync(2); issue(4); rd(2);
      WAIT(cA + 1, 4); issue(5);
      STAMP(10);
      if (wg == 255 && w == 0) {
#pragma unroll 1
        for (int i = 0; i < 4; ++i) sy.poll(ws, WS_HELLO + i * 512 + l * 8, tag);
        gflag(ws, l == 0 ? WS_EPOCH : OOB, tag);
      }
#pragma unroll
      for (int ks = 3; ks < 12; ++ks) { vmwait<8>(); sync(ks); issue(ks + 3); rd(ks); }
      issue(15);
      STAMP(13);
      vmwait<12>(); sync(12); rd(12);
      if (w == 0) sy.poll(ws, WS_FLAG + wg * 2048 + 4 * 128 + (lane_id() & 15) * 8, tag);
      vmwait<8>(); sync(13);
      STAMP(14);
      x1_issue(0); x1_issue(1);
      if (w == 0) ARR(cXs);
      rd(13);
      vmwait<16>(); sync(14); rd(14); rd(15);
      const u32 l2 = lane_id();
      if ((l2 & 15) < 2) {
#pragma unroll
        for (int b = 0; b < 3; ++b) lds_st<f32x4>(L_UV + w * 384 + b * 128 + (l2 & 1) * 64 + (l2 >> 4) * 16, (l2 & 33) == 1 ? uv[b] * -LOG2E : uv[b]);
      }
    }
    {
      const u32 l = lane_id(), q = l >> 4, tok = l & 15;
      u32 xa[4], db = opq(L_GD + 24576 + w * 6144 + l * 16), cs = sy.cb;
#pragma unroll
      for (int k = 0; k < 4; ++k) xa[k] = opq(L_X0 + tok * 1024 + (((4 * k + q) ^ tok) << 4));
      auto xf = [&](int t, int ks) { return lds_ld<u32x4>(xa[ks & 3] + (ks >> 2) * 256 + t * 16384); };
      f32x4 acc[2][3] = {};
      u32x4 a[4];
      vmwait<8>(); ARR(cX);
      STAMP(15);
      __builtin_amdgcn_s_setprio(2);
      WAIT(cX, 4);
      STAMP(16);
#pragma unroll
      for (int n = 0; n < 4; ++n) a[n] = xf(0, n);
#pragma unroll 1
      for (u32 s = 0; s < 4; ++s) {
        u32 pk;
        SB();
#pragma unroll
        for (int n = 0; n < 32; ++n) {
          const int t = n >> 4, ks = n & 15;
          if (n == 28) {
            if (s < 3 && rfl(pk) < 4) sy.wait<cX + 1>(cs, 4);
#pragma unroll
            for (int k = 0; k < 4; ++k) xa[k] ^= (L_X0 ^ L_X1);
            SB();
          }
#pragma unroll
          for (int b = 0; b < 3; ++b) acc[t][b] = mfma32(W[ks * 3 + b], a[n & 3], ks == 0 ? z4 : acc[t][b]);
          a[n & 3] = xf(((n + 4) >> 4) & 1, (n + 4) & 15);
          if (n >= 1 && n < 4) lds_st<f32x4>(db + (3 + n - 1) * 1024, acc[1][n - 1]);
          if (n == 4) { sy.add<cB - 1>(cs); db += (s & 1) ? 24576 : -24576; }
          if (n == 12) pk = *(volatile LAS u32*)(sy.pb + 4 * pDf);
          if (n == 16) {
            if (s == 0) { vmwait<0>(); ARR(cX + 1); }
            const u32 tg = (s >> 1) << (s & 1);
            if (rfl(pk) < tg) WAITP(pDf, tg);
          }
          if (n >= 17 && n < 20) lds_st<f32x4>(db + (n - 17) * 1024, acc[0][n - 17]);
          if (n == 24) pk = *(volatile LAS u32*)(cs + 4 * (cX + 1));
          SB();
        }
        cs += 4;
        if (s == 0) STAMP(17); else if (s == 1) STAMP(18); else if (s == 2) STAMP(19);
      }
      STAMP(20);
      const u32 l2 = lane_id(), ti = l2 >> 5, bq = (l2 >> 4) & 1, pc = 12 * fg + 3 * w, vb = ti * (4 * H2_RB) + (l2 & 15) * 16;
      WAIT(cS + 1, 4);
      const f32x2 st = lds_ld<f32x2>(L_RS + 768 + ti * 128 + (l2 & 15) * 8);
      const u32 uvb = opq(L_UV + w * 384 + bq * 16);
      u32x2 h[3];
#pragma unroll
      for (int b = 0; b < 3; ++b) {
        f32x4 g, u;
#pragma unroll
        for (int k = 0; k < 4; ++k) { auto t = __builtin_amdgcn_permlane32_swap(uf(acc[0][b][k]), uf(acc[1][b][k]), false, false); g[k] = fu(t[0]); u[k] = fu(t[1]); }
        h[b] = glu(g, u, uvb + b * 128, st[0], st[1]);
      }
      const u32 so = WS_H2 + ((T3 >> 4) + 3) * H2_RB;
      {
        auto ex = __builtin_amdgcn_permlane16_swap(h[0].x, h[1].x, false, false);
        auto ey = __builtin_amdgcn_permlane16_swap(h[0].y, h[1].y, false, false);
        st16_sc1(u32x4{ex[0], ey[0], ex[1], ey[1]}, ws, pc + bq < 172 ? vb + (pc + bq) * 256 : OOB, so);
        ex = __builtin_amdgcn_permlane16_swap(h[2].x, h[2].x, false, false);
        ey = __builtin_amdgcn_permlane16_swap(h[2].y, h[2].y, false, false);
        st16_sc1(u32x4{ex[0], ey[0], ex[1], ey[1]}, ws, (bq == 0 && pc + 2 < 172) ? vb + (pc + 2) * 256 : OOB, so);
      }
      __builtin_amdgcn_s_setprio(0);
      STAMP(21);
      const u32 pv = WS_FLAG + wg * 2048 + (9 + 2 * w) * 128 + (l2 < (w == 1 ? 5 : 4) ? l2 : 0) * 8;
      const bool ok = sy.poll_once(ws, pv, tag);
      publish_h2(3, LAST(cPh + 3));
      STAMP(22);
      if (!ok) sy.poll(ws, pv, tag);
      ARRP(pF);
      sy.poll(ws, pv + 64, tag);
      ARRP(pF);
      STAMP(23);
    }
  }
  { u64 clk1; asm volatile("s_memtime %0\n\ts_waitcnt lgkmcnt(0)" : "=s"(clk1) :: "memory"); STAMP(46);
    u32 t0 = __builtin_amdgcn_readlane(stamps, 0), t1 = __builtin_amdgcn_readlane(stamps, 46);
    u32 mhz10 = (u32)(clk1 - clk0) * 10u / (t1 - t0);
    asm volatile("v_writelane_b32 %0, %1, 47" : "+v"(stamps) : "s"(rfl(t0 + mhz10))); }
  { const u32 l = lane_id(); __builtin_amdgcn_raw_buffer_store_b32(stamps, ws, l < NSTAMP ? ((wg * 8 + wave) * NSTAMP + l) * 4 : OOB, WS_STAMP, 0); }
}

__global__ void k_spin(const unsigned* st) {
  __shared__ unsigned mn, mx;
  if (threadIdx.x == 0) { mn = 0xffffffffu; mx = 0; }
  __syncthreads();
  const unsigned b = st[0] - 100000u;
  for (int v = 0; v < 8; ++v) atomicMin(&mn, st[(threadIdx.x * 8 + v) * NSTAMP] - b);
  for (int v = 0; v < 4; ++v) { unsigned z = st[(threadIdx.x * 8 + v) * NSTAMP + 0]; if (z) atomicMax(&mx, z - b); }
  __syncthreads();
  if (threadIdx.x == 0) { u64 d = mx - mn; if (d > 10000) d = 10000; u64 t0 = __builtin_amdgcn_s_memrealtime();
    while (__builtin_amdgcn_s_memrealtime() - t0 < d * 100) __builtin_amdgcn_s_sleep(8); }
}
extern "C" void kernel_launch(void* const* d_in, const int* in_sizes, int n_in, void* d_out, int out_size, void* d_ws, size_t ws_size,
                              hipStream_t stream) {
  Params p;
  p.x = d_in[0]; p.wqkv = d_in[1]; p.wo = d_in[2]; p.wg = d_in[4]; p.wu = d_in[5]; p.wd = d_in[6]; p.c = d_in[7]; p.wmod = d_in[8];
  p.out = d_out; p.ws = d_ws;
  adaln_block<<<256, 512, 0, stream>>>(p);
  k_spin<<<1, 256, 0, stream>>>((const unsigned*)((char*)d_ws + WS_STAMP));
}
```

```cpp
#include <hip/hip_runtime.h>

#define DI __device__ __forceinline__
#define LAS __attribute__((address_space(3)))
typedef unsigned u32;
typedef unsigned long long u64;
typedef u32 u32x2 __attribute__((ext_vector_type(2)));
typedef u32 u32x4 __attribute__((ext_vector_type(4)));
typedef float f32x2 __attribute__((ext_vector_type(2)));
typedef float f32x4 __attribute__((ext_vector_type(4)));
typedef short s16x4 __attribute__((ext_vector_type(4)));
typedef __bf16 bf16x2 __attribute__((ext_vector_type(2)));
typedef __bf16 bf16x8 __attribute__((ext_vector_type(8)));
typedef __amdgpu_buffer_rsrc_t srd_t;

constexpr u32 WS_EPOCH = 0, WS_HELLO = 4096, WS_MODP = 8192, WS_FLAG = 65536, WS_STAT = 0x100000, WS_O = 0x200000,
              WS_X1 = 0x400000, WS_H2 = 0x600000, WS_STAMP = 0xC00000, WS_END = 0xD00000;
constexpr u32 H2_RB = 172 * 256;
constexpr u32 OOB = 0x80000000u;
constexpr int SC1 = 16;
constexpr u32 L_H = 0, L_S = 81920, L_XR = 147456, L_JUNK = 157440, L_CNT = 157696, L_SB = 157952, L_RS = 162048, L_G3 = 163072;
constexpr u32 L_UV = L_SB + 2048, L_X0 = L_S + 32768, L_X1 = L_H, L_GD = L_H + 32768;
enum { cW = 0, cWc, cM1, cM2, cM3, cG, cR, cXs, cS, cH = 10, cPo = 12, cPx = 14, cA = 16, cB = 20  , cX = 24, cPh = 30,
       pWo = 0, pDf = 4, pF = 8 };
constexpr u32 CODE_BYTES = 32768;
#define SPIN (1 << 16)
#define SLEEPSTR ""
constexpr float LOG2E = 1.4426950408889634f;

#define STAMP(k) do {} while (0)
#define SB() __builtin_amdgcn_sched_barrier(0)

DI u32 rfl(u32 x) { return __builtin_amdgcn_readfirstlane(x); }
DI u32 opq(u32 x) { asm volatile("" : "+v"(x)); return x; }
DI u32 lane_id() { u32 l; asm volatile("v_mbcnt_lo_u32_b32 %0, -1, 0\n\tv_mbcnt_hi_u32_b32 %0, -1, %0" : "=v"(l)); return l; }
DI float fu(u32 x) { return __builtin_bit_cast(float, x); }
DI u32 uf(float x) { return __builtin_bit_cast(u32, x); }
DI float bflo(u32 v) { return fu(v << 16); }
DI float bfhi(u32 v) { return fu(v & 0xffff0000u); }
DI u32 cvtpk(float lo, float hi) { f32x2 v = {lo, hi}; return __builtin_bit_cast(u32, __builtin_convertvector(v, bf16x2)); }
DI u32x2 pack4(f32x4 v) { return u32x2{cvtpk(v[0], v[1]), cvtpk(v[2], v[3])}; }
DI u32x4 pack8(f32x4 a, f32x4 b) { return u32x4{cvtpk(a[0], a[1]), cvtpk(a[2], a[3]), cvtpk(b[0], b[1]), cvtpk(b[2], b[3])}; }
DI f32x4 unp4(u32 a, u32 b) { return f32x4{bflo(a), bfhi(a), bflo(b), bfhi(b)}; }
DI float rbf(float x) { return bflo(cvtpk(x, x)); }
DI float dot2(u32 a, u32 b, float c) { return __builtin_amdgcn_fdot2_f32_bf16(__builtin_bit_cast(bf16x2, a), __builtin_bit_cast(bf16x2, b), c, false); }
DI f32x4 mfma32(u32x4 a, u32x4 b, f32x4 c) {
  return __builtin_amdgcn_mfma_f32_16x16x32_bf16(__builtin_bit_cast(bf16x8, a), __builtin_bit_cast(bf16x8, b), c, 0, 0, 0);
}
DI f32x4 mfma16(u32x2 a, u32x2 b, f32x4 c) {
  return __builtin_amdgcn_mfma_f32_16x16x16bf16_1k(__builtin_bit_cast(s16x4, a), __builtin_bit_cast(s16x4, b), c, 0, 0, 0);
}
template <int C> DI float dpp(float x) { return fu(__builtin_amdgcn_update_dpp(0, (int)uf(x), C, 0xf, 0xf, true)); }
DI float max2(float a, float b) { float r; asm("v_max_f32 %0, %1, %2" : "=v"(r) : "v"(a), "v"(b)); return r; }
DI float sum16(float x) { auto r = __builtin_amdgcn_permlane16_swap(uf(x), uf(x), false, false); return fu(r[0]) + fu(r[1]); }
DI float sum32(float x) { auto r = __builtin_amdgcn_permlane32_swap(uf(x), uf(x), false, false); return fu(r[0]) + fu(r[1]); }
DI float max16(float x) { auto r = __builtin_amdgcn_permlane16_swap(uf(x), uf(x), false, false); return fmaxf(fu(r[0]), fu(r[1])); }
DI float max32(float x) { auto r = __builtin_amdgcn_permlane32_swap(uf(x), uf(x), false, false); return fmaxf(fu(r[0]), fu(r[1])); }

template <class T> DI T lds_ld(u32 a) { return *(LAS T*)a; }
template <class T> DI void lds_st(u32 a, T v) { *(LAS T*)a = v; }
DI u32x4 tr_frag(u32 a, u32 hstep) {
  s16x4 lo = __builtin_amdgcn_ds_read_tr16_b64_v4i16((LAS s16x4*)a);
  s16x4 hi = __builtin_amdgcn_ds_read_tr16_b64_v4i16((LAS s16x4*)(a + hstep));
  u32x2 l = __builtin_bit_cast(u32x2, lo), h = __builtin_bit_cast(u32x2, hi);
  return u32x4{l.x, l.y, h.x, h.y};
}
DI void dma2(srd_t r, u32 lds, u32 v0, u32 v1, u32 so) {
  asm volatile("s_mov_b32 m0, %0\n\ts_nop 0\n\tbuffer_load_dwordx4 %1, %3, %4 offen lds\n\tbuffer_load_dwordx4 %2, %3, %4 offen offset:1024 lds"
               ::"s"(lds), "v"(v0), "v"(v1), "s"(r), "s"(so) : "memory");
}
DI void dma4(srd_t r, u32 lds, u32 v0, u32 v1, u32 s0, u32 s1) {
  asm volatile("s_mov_b32 m0, %0\n\ts_nop 0\n\tbuffer_load_dwordx4 %1, %3, %4 offen lds\n\tbuffer_load_dwordx4 %2, %3, %4 offen offset:1024 lds\n\t"
               "buffer_load_dwordx4 %1, %3, %5 offen offset:2048 lds\n\tbuffer_load_dwordx4 %2, %3, %5 offen offset:3072 lds"
               ::"s"(lds), "v"(v0), "v"(v1), "s"(r), "s"(s0), "s"(s1) : "memory");
}
DI void dma4_sc1(srd_t r, u32 lds, u32 v0, u32 so) {
  asm volatile("s_mov_b32 m0, %0\n\ts_nop 0\n\tbuffer_load_dwordx4 %1, %5, %6 offen sc1 lds\n\tbuffer_load_dwordx4 %2, %5, %6 offen offset:1024 sc1 lds\n\t"
               "buffer_load_dwordx4 %3, %5, %6 offen offset:2048 sc1 lds\n\tbuffer_load_dwordx4 %4, %5, %6 offen offset:3072 sc1 lds"
               ::"s"(lds), "v"(v0), "v"(v0 ^ 16), "v"(v0 ^ 32), "v"(v0 ^ 48), "s"(r), "s"(so) : "memory");
}
DI void touch(srd_t r, u32 voff, u32 soff) {
  asm volatile("s_mov_b32 m0, %0\n\ts_nop 0\n\tbuffer_load_dword %1, %2, %3 offen sc1 lds" ::"s"(L_JUNK), "v"(voff), "s"(r), "s"(soff) : "memory");
}
template <int N> DI void vmwait() { asm volatile("s_waitcnt vmcnt(%0)" ::"i"(N) : "memory"); }
DI void lgwait() { asm volatile("s_waitcnt lgkmcnt(0)" ::: "memory"); }
DI void st16_sc1(u32x4 v, srd_t r, u32 voff, u32 soff) {
  asm volatile("buffer_store_dwordx4 %0, %1, %2, %3 offen sc1\n\ts_nop 1" ::"v"(v), "v"(voff), "s"(r), "s"(soff) : "memory");
}
DI void st16(u32x4 v, srd_t r, u32 voff, u32 soff) {
  asm volatile("buffer_store_dwordx4 %0, %1, %2, %3 offen\n\ts_nop 1" ::"v"(v), "v"(voff), "s"(r), "s"(soff) : "memory");
}
DI void gflag(srd_t r, u32 voff, u64 tag) { __builtin_amdgcn_raw_buffer_store_b64(u32x2{(u32)tag, (u32)(tag >> 32)}, r, voff, 0, SC1); }

struct Sync {
  u32 cb, pb, one; int n;
  template <int I> DI void add(u32 a) {
    asm volatile("s_mov_b64 exec, 1\n\tds_add_u32 %0, %1 offset:%2\n\ts_mov_b64 exec, -1" ::"v"(a), "v"(one), "i"(4 * I) : "memory");
  }
  template <int I> DI u32 add_rtn(u32 a) {
    u32 v, s;
    asm volatile("s_mov_b64 exec, 1\n\tds_add_rtn_u32 %0, %2, %3 offset:%4\n\ts_mov_b64 exec, -1\n\ts_waitcnt lgkmcnt(0)\n\tv_readfirstlane_b32 %1, %0"
                 : "=&v"(v), "=s"(s) : "v"(a), "v"(one), "i"(4 * I) : "memory");
    return s;
  }
  template <int I> DI void wait(u32 a, u32 target) {
    u32 v, s;
    asm volatile("1:\n\tds_read_b32 %0, %3 offset:%5\n\ts_waitcnt lgkmcnt(0)\n\tv_readfirstlane_b32 %1, %0\n\ts_cmp_ge_u32 %1, %4\n\ts_cbranch_scc1 2f\n\t"
                 SLEEPSTR "s_sub_i32 %2, %2, 1\n\ts_cmp_gt_i32 %2, 0\n\ts_cbranch_scc1 1b\n2:"
                 : "=&v"(v), "=&s"(s), "+s"(n) : "v"(a), "s"(target), "i"(4 * I) : "memory", "scc");
  }
  DI void poll(srd_t r, u32 voff, u64 tag) {
    u64 v;
    asm volatile("1:\n\tbuffer_load_dwordx2 %0, %2, %3, 0 offen sc1\n\ts_waitcnt vmcnt(0)\n\tv_cmp_ne_u64 vcc, %4, %0\n\ts_cmp_eq_u64 vcc, 0\n\t"
                 "s_cbranch_scc1 2f\n\ts_sub_i32 %1, %1, 8\n\ts_cmp_gt_i32 %1, 0\n\ts_cbranch_scc1 1b\n2:"
                 : "=&v"(v), "+s"(n) : "v"(voff), "s"(r), "s"(tag) : "memory", "scc", "vcc");
  }
  DI bool poll_once(srd_t r, u32 voff, u64 tag) {
    u64 v, m;
    asm volatile("buffer_load_dwordx2 %0, %2, %3, 0 offen sc1\n\ts_waitcnt vmcnt(0)\n\tv_cmp_ne_u64 %1, %4, %0"
                 : "=&v"(v), "=s"(m) : "v"(voff), "s"(r), "s"(tag) : "memory");
    return m == 0;
  }
  template <bool X> DI f32x4 gather_mod(srd_t ws, u32 col, u64 tag, u32 xo, f32x4& xr) {
    const u32 t0 = tag & 1023, t1 = (tag >> 10) & 1023, t2 = (tag >> 20) & 1023, t3 = (tag >> 30) & 1023;
    u32x4 p[5];
    do {
      asm volatile("" ::: "memory");
      u32 bad = 0;
#pragma unroll
      for (int k = 0; k < 4; ++k) p[k] = __builtin_amdgcn_raw_buffer_load_b128(ws, col * 4, WS_MODP + k * 12288, SC1);
      if (X) p[4] = __builtin_amdgcn_raw_buffer_load_b128(ws, xo, WS_MODP, SC1);
      SB();
#pragma unroll
      for (int k = 0; k < (X ? 5 : 4); ++k) bad |= (p[k].x ^ t0) | (p[k].y ^ t1) | (p[k].z ^ t2) | (p[k].w ^ t3);
      if (__builtin_amdgcn_ballot_w64((bad & 1023) != 0) == 0) break;
      n -= 8;
    } while (n > 0);
    f32x4 r;
#pragma unroll
    for (int j = 0; j < 4; ++j) {
      r[j] = rbf((fu(p[0][j] & ~1023u) + fu(p[1][j] & ~1023u)) + (fu(p[2][j] & ~1023u) + fu(p[3][j] & ~1023u)));
      if (X) xr[j] = rbf(sum32(sum16(fu(p[4][j] & ~1023u))));
    }
    return r;
  }
};
#define ARR(i) sy.add<i>(sy.cb)
#define ARRP(i) sy.add<i>(sy.pb)
#define LAST(i) (sy.add_rtn<i>(sy.cb) == 3)
#define WAIT(i, t) sy.wait<i>(sy.cb, t)
#define WAITP(i, t) sy.wait<i>(sy.pb, t)

struct Params {
  const void *x, *wqkv, *wo, *wg, *wu, *wd, *c, *wmod;
  void *out, *ws;
};

__global__ void __launch_bounds__(512, 2) adaln_block(Params P) {
  __shared__ __attribute__((aligned(16))) char smem[163840];
  const u32 wave = rfl(threadIdx.x >> 6), w = wave & 3;
  const u32 wg = blockIdx.x, xcd = wg & 7, r = wg >> 3, tq = r >> 3, cg = r & 7, fg = r & 15;
  const u32 T1 = xcd * 256 + tq * 64, T3 = xcd * 256 + (r >> 4) * 128;
  asm volatile("" ::"s"((u32)(size_t)(LAS char*)smem) : "memory");
  STAMP(0);
  if (wave == 4) {
    u64 pc;
    asm volatile("s_getpc_b64 %0" : "=s"(pc));
    const u32 l = lane_id();
    touch(__builtin_amdgcn_make_buffer_rsrc((void*)(pc & ~127ull), 0, CODE_BYTES, 0x00020000), (l * 32 + r) * 128, 0);
    lds_st<u32>(L_CNT + 4 * l, 0u);
    lgwait();
  }
  const srd_t ws = __builtin_amdgcn_make_buffer_rsrc(P.ws, 0, WS_END, 0x00020000);
  const srd_t rx = __builtin_amdgcn_make_buffer_rsrc((void*)P.x, 0, 2048 * 1024, 0x00020000);
  const srd_t rqkv = __builtin_amdgcn_make_buffer_rsrc((void*)P.wqkv, 0, 512 * 3072, 0x00020000);
  const f32x4 z4 = {0.f, 0.f, 0.f, 0.f};
  Sync sy;
  sy.cb = opq(L_CNT); sy.pb = opq(L_CNT + 160 + 4 * w); sy.one = opq(1); sy.n = SPIN;
  u64 tag;
  auto x1_issue = [&](int s) {
    const u32 l = lane_id(), xv = fg == 15 ? OOB : (((l ^ (8 * (w & 1))) + 8 * xcd) & 63) * 16;
    const u32 d = ((s & 1) ? L_X1 : L_X0) + w * 8192, so = WS_X1 + (T3 + 64 * (w >> 1) + 16 * s + 8 * (w & 1)) * 1024;
    dma4_sc1(ws, d, xv, so); dma4_sc1(ws, d + 4096, xv ^ 64, so + 4096);
  };
  auto glu = [&](f32x4 g, f32x4 u, u32 uvb, float rs, float mu) {
    const f32x4 ug = lds_ld<f32x4>(uvb), uu = lds_ld<f32x4>(uvb + 32), vg = lds_ld<f32x4>(uvb + 64), vu = lds_ld<f32x4>(uvb + 96);
    const float rn = rs * -LOG2E;
    f32x4 h;
#pragma unroll
    for (int k = 0; k < 4; ++k) {
      const float e = __builtin_amdgcn_exp2f(fmaf(rn, fmaf(-mu, ug[k], g[k]), vg[k]));
      h[k] = fmaf(rs, fmaf(-mu, uu[k], u[k]), vu[k]) * __builtin_amdgcn_rcpf(1.f + e);
    }
    return pack4(h);
  };
  auto publish_h2 = [&](int s, bool last) {
    const u32 l = lane_id();
    if (last) {
      const u32 w1 = (12 * fg) / 44, w2 = (12 * fg + 11) / 44, wc = l < 16 ? w1 : w2, first = wc == 0 ? 0 : 4 * wc - 1;
      const bool on = (l < 16 || (l < 32 && w2 != w1)) && wc < 4;
      gflag(ws, on ? WS_FLAG + (((r & 16) + (l & 15)) * 8 + xcd) * 2048 + (8 + 2 * wc + (s >> 1)) * 128 + (s & 1) * 64 + (fg - first) * 8 : OOB, tag);
    }
    const u32 ln = (12 * fg + 3 * w) * 2 + (l & 7);
    touch(ws, (l < 16 && (l & 7) < 6 && ln < 344) ? (l >> 3) * (4 * H2_RB) + ln * 128 : OOB, WS_H2 + ((T3 >> 4) + s) * H2_RB);
  };

  if (wave < 4) {
    u32x4 W[48];
    f32x4 x1r[2];
    {
      const u32 l = lane_id(), q = l >> 4;
      const srd_t rm = __builtin_amdgcn_make_buffer_rsrc((void*)P.wmod, 0, 512 * 6144, 0x00020000);
      const srd_t rc = __builtin_amdgcn_make_buffer_rsrc((void*)P.c, 0, 1024, 0x00020000);
      const u32 kq = wg / 48, lc = wg - kq * 48, k0 = 128 * kq + 32 * w + 4 * (l >> 3);
      const u32 mv0 = k0 * 6144 + lc * 128 + (l & 7) * 16;
      u32x4 wm[4];
      const u32x2 cv = __builtin_amdgcn_raw_buffer_load_b64(rc, k0 * 2, 0, 0);
#pragma unroll
      for (int i = 0; i < 4; ++i) wm[i] = __builtin_amdgcn_raw_buffer_load_b128(rm, mv0 + i * 6144, 0, 0);
      const u32x2 ep = __builtin_amdgcn_raw_buffer_load_b64(ws, 0, WS_EPOCH, SC1);
      SB();
      __builtin_amdgcn_s_barrier();
      asm volatile("" ::: "memory");
      SB();
      STAMP(32);
      if (kq < 4) {
        float sc[4] = {bflo(cv.x), bfhi(cv.x), bflo(cv.y), bfhi(cv.y)}, a[8] = {};
#pragma unroll
        for (int i = 0; i < 4; ++i) {
          const float s = rbf(sc[i] * __builtin_amdgcn_rcpf(1.f + __builtin_amdgcn_exp2f(sc[i] * -LOG2E)));
#pragma unroll
          for (int j = 0; j < 4; ++j) { a[2 * j] = fmaf(s, bflo(wm[i][j]), a[2 * j]); a[2 * j + 1] = fmaf(s, bfhi(wm[i][j]), a[2 * j + 1]); }
        }
        float b[4], d[2];
#pragma unroll
        for (int j = 0; j < 4; ++j) { auto t = __builtin_amdgcn_permlane32_swap(uf(a[j]), uf(a[j + 4]), false, false); b[j] = fu(t[0]) + fu(t[1]); }
#pragma unroll
        for (int j = 0; j < 2; ++j) { auto t = __builtin_amdgcn_permlane16_swap(uf(b[j]), uf(b[j + 2]), false, false); d[j] = fu(t[0]) + fu(t[1]); }
        d[0] += dpp<0x128>(d[0]); d[1] += dpp<0x128>(d[1]);
        lds_st<f32x2>(L_RS + w * 256 + ((l & 7) * 8 + (l >> 5) * 4 + (q & 1) * 2) * 4, f32x2{d[0], d[1]});
      }
      tag = ((u64)rfl(ep.x) | ((u64)rfl(ep.y) << 32)) + 0x40100401ull;
      asm volatile("" ::"s"(tag));
      STAMP(33);
      if (kq < 4 && LAST(cM1)) {
        const u32 o = L_RS + (l & 15) * 16;
        const f32x4 s = (lds_ld<f32x4>(o) + lds_ld<f32x4>(o + 256)) + (lds_ld<f32x4>(o + 512) + lds_ld<f32x4>(o + 768));
        u32x4 e;
#pragma unroll
        for (int j = 0; j < 4; ++j) e[j] = ((uf(s[j]) + 512u) & ~1023u) | ((u32)(tag >> (10 * j)) & 1023u);
        st16_sc1(e, ws, l < 16 ? l * 16 : OOB, WS_MODP + kq * 12288 + lc * 256);
      }
      STAMP(34);
    }
    {
      const u32 l = lane_id(), q = l >> 4, tok = l & 15, jj = (l >> 2) & 3, drow = l >> 3;
      const u32 v0 = opq((16 * w + drow) * 3072 + cg * 128 + ((l & 7) ^ (drow & 2)) * 16), v1 = opq((v0 ^ 64) + 8 * 3072 - 1024);
      auto issue = [&](int c) {
        const u32 so = ((c + xcd) & 7) * (64 * 3072), d = L_S + (c % 3) * 24576 + w * 2048;
#pragma unroll
        for (int s = 0; s < 3; ++s) dma2(rqkv, d + s * 8192, v0, v1, so + s * 1024);
      };
      issue(0); issue(1); issue(2);
      const u32 tb = opq(L_S + (8 * q + jj) * 128 + (l & 3) * 8 + ((w ^ (jj >> 1) ^ ((q & 1) << 1)) << 5));
      u32 e0 = opq(q * 256 + ((tok ^ q) << 4)), o0 = opq((e0 ^ 64) + 1024);
      auto rd = [&](int c) {
        const u32 t = c % 3 == 2 ? opq(tb + 49152) : tb + (c % 3) * 24576;
#pragma unroll
        for (int kk = 0; kk < 2; ++kk)
#pragma unroll
          for (int s = 0; s < 3; ++s) W[(2 * c + kk) * 3 + s] = tr_frag(t + s * 8192 + kk * 4096, 512);
      };
      auto chunk = [&](int c) {
        if (c == 0) vmwait<12>(); else if (c < 7) vmwait<6>(); else vmwait<0>();
        ARR(cW); WAIT(cW, 4 * c + 4);
        if (c >= 1 && c < 6) issue(c + 2);
        rd(c);
        if (c >= 5) ARR(cWc);
        SB();
      };
      auto hf = [&](int rb, int ks) { return lds_ld<u32x4>(((ks & 1) ? o0 : e0) + rb * 16384 + (ks >> 1) * 2048); };
      chunk(0); STAMP(1); chunk(1); chunk(2); chunk(3);
      STAMP(2);
      WAIT(cH, 4);
      __builtin_amdgcn_s_setprio(2);
      STAMP(3);
      u32x4 a[4];
      u32x2 qb[2], kb[3], vb[3];
      {
        f32x4 aq[2] = {}, ak[3] = {}, av[3] = {};
#pragma unroll
        for (int n = 0; n < 4; ++n) a[n] = hf(n % 3, n / 3);
        SB();
#pragma unroll
        for (int n = 0; n < 48; ++n) {
          const int ks = n / 3, rb = n % 3;
          if (n == 12) chunk(4);
          if (n == 24) chunk(5);
          if (n == 36) chunk(6);
          if (n == 42) { chunk(7); STAMP(4); }
          if (rb) aq[rb - 1] = mfma32(W[ks * 3], a[n & 3], aq[rb - 1]);
          ak[rb] = mfma32(W[ks * 3 + 1], a[n & 3], ak[rb]); av[rb] = mfma32(a[n & 3], W[ks * 3 + 2], av[rb]);
          if (n + 4 < 48) a[n & 3] = hf((n + 4) % 3, (n + 4) / 3);
          SB();
        }
        ARR(cA);
#pragma unroll
        for (int k = 0; k < 3; ++k) { kb[k] = pack4(ak[k]); vb[k] = pack4(av[k]); }
        qb[0] = pack4(aq[0]); qb[1] = pack4(aq[1]);
      }
      auto attn = [&](int j) {
        const u32 l = lane_id(), q = l >> 4, tok = l & 15, par = tok & 1;
        const float ninf = -__builtin_inff();
        const bool c0 = 4 * q + par <= tok, c1 = 4 * q + 2 + par <= tok;
        f32x4 bc, bp;
#pragma unroll
        for (int k = 0; k < 4; ++k) { const bool pm = (k & 1) == par, cu = k < 2 ? c0 : c1; bc[k] = pm && cu ? 0.f : ninf; bp[k] = pm && !cu ? 0.f : ninf; }
        const u32 s0 = par ? 0x01000c0cu : 0x0c0c0100u, s1 = par ? 0x03020c0cu : 0x0c0c0302u, zz = 0x0c0c0c0cu;
        const u32 sp0 = c0 ? zz : s0, sp1 = c1 ? zz : s1, sc0 = c0 ? s0 : zz, sc1 = c1 ? s1 : zz;
        u32x2 ob[2];
#pragma unroll
        for (int t = 0; t < 2; ++t) {
          f32x4 b2 = bp;
          if (j == 0 && t == 0 && T1 == 0) b2 = f32x4{ninf, ninf, ninf, ninf};
          const f32x4 sc = mfma16(kb[t + 1], qb[t], bc), sp = mfma16(kb[t], qb[t], b2);
          const float e0 = fmaxf(fmaxf(sc[0], sc[1]), fmaxf(sp[0], sp[1])), e1 = fmaxf(fmaxf(sc[2], sc[3]), fmaxf(sp[2], sp[3]));
          const float cs = 0.25f * LOG2E, mx = max32(max16(fmaxf(e0, e1))) * cs;
          const float p0 = __builtin_amdgcn_exp2f(fmaf(e0, cs, -mx)), p1 = __builtin_amdgcn_exp2f(fmaf(e1, cs, -mx));
          const u32 pk = cvtpk(p0, p1);
          const u32x4 pb = {__builtin_amdgcn_perm(0u, pk, sp0), __builtin_amdgcn_perm(0u, pk, sp1), __builtin_amdgcn_perm(0u, pk, sc0), __builtin_amdgcn_perm(0u, pk, sc1)};
          const f32x4 o = mfma32(u32x4{vb[t].x, vb[t].y, vb[t + 1].x, vb[t + 1].y}, pb, z4);
          ob[t] = pack4(o * __builtin_amdgcn_rcpf(sum32(sum16(p0 + p1))));
        }
        auto ex = __builtin_amdgcn_permlane16_swap(ob[0].x, ob[1].x, false, false);
        auto ey = __builtin_amdgcn_permlane16_swap(ob[0].y, ob[1].y, false, false);
        st16_sc1(u32x4{ex[0], ey[0], ex[1], ey[1]}, ws, (q & 1) * 16384 + (q >> 1) * 256 + tok * 16, WS_O + ((T1 >> 4) + 2 * j) * 16384 + (4 * cg + w) * 512);
      };
      auto publish_o = [&](int j, bool last) {
        const u32 l = lane_id();
        if (last) gflag(ws, l < 8 ? WS_FLAG + ((tq * 8 + l) * 8 + xcd) * 2048 + (cg >> 1) * 128 + (cg & 1) * 8 + j * 16 : OOB, tag);
        touch(ws, l < 8 ? (l >> 2) * 16384 + (l & 3) * 128 : OOB, WS_O + ((T1 >> 4) + 2 * j) * 16384 + (4 * cg + w) * 512);
      };
      STAMP(5);
      attn(0);
      STAMP(6);
      WAIT(cH + 1, 4);
      {
        e0 += 49152; o0 += 49152;
        f32x4 aq[2] = {}, ak[2] = {}, av[2] = {};
#pragma unroll
        for (int n = 0; n < 4; ++n) a[n] = hf(n & 1, n >> 1);
        SB();
#pragma unroll
        for (int n = 0; n < 32; ++n) {
          const int ks = n >> 1, rb = n & 1;
          if (n == 16) { vmwait<0>(); publish_o(0, LAST(cPo)); STAMP(27); SB(); }
          aq[rb] = mfma32(W[ks * 3], a[n & 3], aq[rb]); ak[rb] = mfma32(W[ks * 3 + 1], a[n & 3], ak[rb]); av[rb] = mfma32(a[n & 3], W[ks * 3 + 2], av[rb]);
          if (n + 4 < 32) a[n & 3] = hf(n & 1, (n + 4) >> 1);
          SB();
        }
        ARR(cA + 1);
        kb[0] = kb[2]; vb[0] = vb[2];
#pragma unroll
        for (int k = 0; k < 2; ++k) { kb[k + 1] = pack4(ak[k]); vb[k + 1] = pack4(av[k]); qb[k] = pack4(aq[k]); }
      }
      STAMP(35);
      attn(1);
      __builtin_amdgcn_s_setprio(0);
      STAMP(36);
      const u32 l2 = lane_id();
      {
      const u32 l = l2, q = l >> 4, tok = l & 15, jj = (l >> 2) & 3;
      const u32 c8 = l & 7, rtk = 8 * (w & 1) + (l >> 3);
      const u32 ev = opq((8 * w + (l >> 3)) * 1024 + c8 * 16);
      u32x4 xres[2];
#pragma unroll
      for (int j = 0; j < 2; ++j) xres[j] = __builtin_amdgcn_raw_buffer_load_b128(rx, ev, (T1 + 32 * j) * 1024 + cg * 128, 0);
      SB();
      const u32 tb = L_S + w * 4096 + (8 * q + jj) * 128 + (l & 3) * 8 + (((jj >> 1) ^ ((q & 1) << 1)) << 5);
      WAITP(pWo, 1);
#pragma unroll
      for (int cb = 0; cb < 4; ++cb) {
        const u32 b = opq(tb ^ (cb << 5));
#pragma unroll
        for (int i = 0; i < 4; ++i) W[i * 4 + cb] = tr_frag(b + i * 16384, 512);
      }
      ARR(cR);
      const u32 pv = opq(WS_FLAG + wg * 2048 + w * 128 + (l & 1) * 8);
      bool ok = sy.poll_once(ws, pv, tag);
      publish_o(1, LAST(cPo + 1));
      const u32 wa = opq(L_S + w * 4096 + tok * 256 + ((q ^ (tok & 7)) << 4)), wb = opq(wa ^ 64);
      const u32 r0 = opq(L_S + (w >> 1) * 16384 + rtk * 256 + (((2 * c8) ^ (rtk & 7)) << 4)), r1 = opq(r0 ^ 16);
      const u32 l16 = opq(l * 16), sv = opq(c8 == 0 ? rtk * 8 : OOB);
      const u32 fv = opq(l < 16 ? WS_FLAG + (((r & 16) + l) * 8 + xcd) * 2048 + 4 * 128 + fg * 8 : OOB);
      STAMP(7);
#pragma unroll
      for (int j = 0; j < 2; ++j) {
        if (!ok) sy.poll(ws, pv + j * 16, tag);
        if (j == 0) STAMP(8); else STAMP(12);
        u32x4 a[2][4];
#pragma unroll
        for (int i = 0; i < 4; ++i)
#pragma unroll
          for (int rb = 0; rb < 2; ++rb)
            a[rb][i] = __builtin_amdgcn_raw_buffer_load_b128(ws, l16, WS_O + ((T1 >> 4) + 2 * j + rb) * 16384 + (4 * w + ((i + xcd) & 3)) * 1024, SC1);
        SB();
        if (j == 0) WAIT(cM3, 4);
        WAIT(cR, 4 + 8 * j);
        const u32 g3 = L_G3 + c8 * 32;
        const f32x4 ga0 = lds_ld<f32x4>(g3), ga1 = lds_ld<f32x4>(g3 + 16), s0 = lds_ld<f32x4>(g3 + 256), s1 = lds_ld<f32x4>(g3 + 272);
        SB();
        f32x4 acc[2][4] = {};
#pragma unroll
        for (int i = 0; i < 4; ++i)
#pragma unroll
          for (int rb = 0; rb < 2; ++rb) {
#pragma unroll
            for (int cb = 0; cb < 4; ++cb) acc[rb][cb] = mfma32(W[i * 4 + cb], a[rb][i], acc[rb][cb]);
            SB();
          }
#pragma unroll
        for (int rb = 0; rb < 2; ++rb) {
          lds_st<f32x4>(wa + rb * 16384, acc[rb][0]); lds_st<f32x4>(wb + rb * 16384, acc[rb][1]);
          lds_st<f32x4>(wa + rb * 16384 + 128, acc[rb][2]); lds_st<f32x4>(wb + rb * 16384 + 128, acc[rb][3]);
        }
        ARR(cR);
        if (j == 0) STAMP(9); else STAMP(13);
        WAIT(cR, 8 + 8 * j);
        const f32x4 y0 = (lds_ld<f32x4>(r0) + lds_ld<f32x4>(r0 + 4096)) + (lds_ld<f32x4>(r0 + 8192) + lds_ld<f32x4>(r0 + 12288));
        const f32x4 y1 = (lds_ld<f32x4>(r1) + lds_ld<f32x4>(r1 + 4096)) + (lds_ld<f32x4>(r1 + 8192) + lds_ld<f32x4>(r1 + 12288));
        ARR(cR);
        const f32x4 u0 = unp4(xres[j].x, xres[j].y) + ga0 * y0, u1 = unp4(xres[j].z, xres[j].w) + ga1 * y1;
        st16_sc1(pack8(u0 * s0, u1 * s1), ws, ev, WS_X1 + (T1 + 32 * j) * 1024 + cg * 128);
        const f32x4 t1 = u0 + u1, t2 = u0 * u0 + u1 * u1;
        float sm = (t1[0] + t1[1]) + (t1[2] + t1[3]), sq = (t2[0] + t2[1]) + (t2[2] + t2[3]);
        sm += dpp<0xB1>(sm); sq += dpp<0xB1>(sq);
        sm += dpp<0x4E>(sm); sq += dpp<0x4E>(sq);
        sm += dpp<0x141>(sm); sq += dpp<0x141>(sq);
        __builtin_amdgcn_raw_buffer_store_b64(u32x2{uf(sm), uf(sq)}, ws, sv, WS_STAT + (((T1 >> 4) + 2 * j + (w >> 1)) * 8 + cg) * 128, SC1);
        if (j == 0) { lds_st<f32x4>(L_XR + w * 2048 + l16, u0); lds_st<f32x4>(L_XR + w * 2048 + 1024 + l16, u1); }
        else { x1r[0] = u0; x1r[1] = u1; }
        if (j == 0) STAMP(10); else STAMP(14);
        if (j == 0) ok = sy.poll_once(ws, pv + 16, tag); else vmwait<0>();
        if (j == 0 ? LAST(cPx) : LAST(cPx + 1)) gflag(ws, fv + j * 128, tag);
        touch(ws, c8 == 0 ? ev : OOB, WS_X1 + (T1 + 32 * j) * 1024 + cg * 128);
        if (j == 0) STAMP(11); else STAMP(15);
      }
      }
    }
    {
      const srd_t rd = __builtin_amdgcn_make_buffer_rsrc((void*)P.wd, 0, 1376 * 1024, 0x00020000);
      auto kstep = [&](int i) { const u32 a = i + xcd, b = a - 11; return 11 * w + (a < b ? a : b); };
      auto wd_issue = [&](int rnd) {
        const u32 l = lane_id(), v0 = (l >> 3) * 1024 + cg * 128 + ((l & 7) ^ ((l >> 3) & 2)) * 16;
#pragma unroll
        for (int i = 2 * rnd; i < 2 * rnd + 2 && i < 11; ++i) {
          const u32 ks = kstep(i), ve = ks > 42 ? OOB : v0, so = ks > 42 ? 0 : ks * 32768;
          dma4(rd, L_S + w * 8192 + (i & 1) * 4096, ve, (ve ^ 64) + 7168, so, so + 14336);
        }
      };
      auto wd_read = [&](int rnd) {
        const u32 l = lane_id(), q = l >> 4, jj = (l >> 2) & 3;
        const u32 b0 = L_S + w * 8192 + (8 * q + jj) * 128 + (l & 3) * 8 + (((jj >> 1) ^ ((q & 1) << 1)) << 5);
#pragma unroll
        for (int cb = 0; cb < 4; ++cb) {
          const u32 b = opq(b0 ^ (cb << 5));
#pragma unroll
          for (int i = 2 * rnd; i < 2 * rnd + 2 && i < 11; ++i) W[i * 4 + cb] = tr_frag(b + (i & 1) * 4096, 512);
        }
        lgwait();
      };
      auto stats_ld = [&](int j) {
        u32x4 p;
        asm volatile("buffer_load_dwordx4 %0, %1, %2, %3 offen sc1" : "=v"(p)
                     : "v"(lane_id() * 16), "s"(ws), "s"(WS_STAT + ((T3 >> 4) + 4 * (w >> 1) + 2 * j + (w & 1)) * 1024) : "memory");
        return p;
      };
      auto stats = [&](int j, u32x4 p) {
        asm volatile("" : "+v"(p));
        float v[4];
#pragma unroll
        for (int k = 0; k < 4; ++k) { float t = fu(p[k]); t += dpp<0x128>(t); v[k] = sum32(sum16(t)); }
        const float m0 = v[0] * (1.f / 512), m1 = v[2] * (1.f / 512);
        const float r0 = __builtin_amdgcn_rsqf(fmaxf(v[1] * (1.f / 512) - m0 * m0, 0.f) + 1e-5f);
        const float r1 = __builtin_amdgcn_rsqf(fmaxf(v[3] * (1.f / 512) - m1 * m1, 0.f) + 1e-5f);
        lds_st<f32x4>(L_RS + ((2 * j + (w & 1)) * 2 + (w >> 1)) * 128 + (lane_id() & 7) * 16, f32x4{r0, m0, r1, m1});
        if (j == 0) ARR(cS); else ARR(cS + 1);
      };
      u32 d01, d2, uv01, uv2, sta, hv0, hv1;
      {
        const u32 l = lane_id(), ti = l >> 5, bq = (l >> 4) & 1, tok = l & 15, db = L_GD + w * 6144 + ti * 3072 + tok * 16;
        const u32 pc = 12 * fg + 3 * w, vb = ti * (4 * H2_RB) + tok * 16;
        d01 = opq(db + bq * 1024); d2 = opq(db + 2048 + bq * 256); uv01 = opq(L_UV + w * 384 + bq * 128); uv2 = opq(L_UV + w * 384 + 256 + bq * 16);
        sta = opq(L_RS + ti * 128 + tok * 8);
        hv0 = opq(pc + bq < 172 ? vb + (pc + bq) * 256 : OOB); hv1 = opq((bq == 0 && pc + 2 < 172) ? vb + (pc + 2) * 256 : OOB);
      }
      auto epilogue = [&](int s) {
        const u32 o = (s & 1) * 24576, so = WS_H2 + ((T3 >> 4) + s) * H2_RB;
        const f32x2 st = lds_ld<f32x2>(sta + s * 256);
        const f32x4 g0 = lds_ld<f32x4>(d01 + o), u0 = lds_ld<f32x4>(d01 + o + 512), g1 = lds_ld<f32x4>(d01 + o + 256), u1 = lds_ld<f32x4>(d01 + o + 768);
        const u32x2 h0 = glu(g0, u0, uv01, st[0], st[1]);
        SB();
        const f32x4 g2 = lds_ld<f32x4>(d2 + o), u2 = lds_ld<f32x4>(d2 + o + 512);
        const u32x2 h1 = glu(g1, u1, uv01 + 16, st[0], st[1]);
        st16_sc1(u32x4{h0.x, h0.y, h1.x, h1.y}, ws, hv0, so);
        ARRP(pDf);
        const u32x2 h2 = glu(g2, u2, uv2, st[0], st[1]);
        auto ex = __builtin_amdgcn_permlane16_swap(h2.x, h2.x, false, false);
        auto ey = __builtin_amdgcn_permlane16_swap(h2.y, h2.y, false, false);
        st16_sc1(u32x4{ex[0], ey[0], ex[1], ey[1]}, ws, hv1, so);
      };
      WAIT(cR, 20);
      WAIT(cXs, 1);
      STAMP(16);
      u32x4 sp = stats_ld(0);
      wd_issue(0);
      vmwait<8>(); stats(0, sp);
      vmwait<0>(); wd_read(0); wd_issue(1);
      if (w == 0) { sy.poll(ws, WS_FLAG + wg * 2048 + 5 * 128 + (lane_id() & 15) * 8, tag); ARR(cXs); }
      vmwait<0>(); wd_read(1);
      WAIT(cXs, 2);
      sp = stats_ld(1);
      wd_issue(2);
      WAIT(cS, 4);
      STAMP(17);
      WAIT(cB, 4); STAMP(18); x1_issue(2); epilogue(0);
      vmwait<2>(); ARR(cX + 2);
      wd_read(2); wd_issue(3);
      stats(1, sp);
      vmwait<8>(); publish_h2(0, LAST(cPh));
      STAMP(19);
      WAIT(cB + 1, 4); STAMP(20); x1_issue(3); epilogue(1);
      vmwait<2>(); ARR(cX + 3);
      wd_read(3); wd_issue(4);
      vmwait<8>(); publish_h2(1, LAST(cPh + 1));
      STAMP(21);
      vmwait<0>(); wd_read(4); wd_issue(5);
      WAIT(cS + 1, 4);
      WAIT(cB + 2, 4); STAMP(22); epilogue(2);
      vmwait<2>(); wd_read(5);
      STAMP(23);
      const srd_t ro = __builtin_amdgcn_make_buffer_rsrc(P.out, 0, 2048 * 1024, 0x00020000);
      const u32 l = lane_id(), l16 = opq(l * 16);
      {
        const u32 pv = WS_FLAG + wg * 2048 + (8 + 2 * w) * 128 + (l & 8) * 8 + ((l & 7) < (w == 1 ? 5 : 4) ? (l & 7) : 0) * 8;
        const bool ok = sy.poll_once(ws, pv, tag);
        publish_h2(2, LAST(cPh + 2));
        if (!ok) sy.poll(ws, pv, tag);
      }
      STAMP(24);
      u32x4 a[8];
      auto h2_ld = [&](int n) {
        const u32 ks = kstep(n % 11);
        a[n & 7] = __builtin_amdgcn_raw_buffer_load_b128(ws, l16, WS_H2 + ((T1 >> 4) + n / 11) * H2_RB + (ks > 42 ? 42 : ks) * 1024, SC1);
      };
#pragma unroll
      for (int n = 0; n < 8; ++n) h2_ld(n);
      SB();
      const u32 c8 = l & 7, rtk = 8 * (w & 1) + (l >> 3);
      const u32 wa = opq(L_X0 + w * 4096 + (l & 15) * 256 + (((l >> 4) ^ (l & 7)) << 4));
      const u32 r0 = opq(L_X0 + (w >> 1) * 16384 + rtk * 256 + (((2 * c8) ^ (rtk & 7)) << 4));
#pragma unroll
      for (int g = 0; g < 4; ++g) {
        f32x4 acc[4] = {};
        SB();
#pragma unroll
        for (int i = 0; i < 11; ++i) {
          const int n = 11 * g + i;
          if (n == 14) { WAITP(pF, 1); SB(); }
#pragma unroll
          for (int cb = 0; cb < 4; ++cb) acc[cb] = mfma32(W[i * 4 + cb], a[n & 7], acc[cb]);
          if (n + 8 < 33 || (g == 3 && n + 8 < 44)) h2_ld(n + 8);
          SB();
        }
        {
          const u32 wb = wa ^ 64, o = (g & 1) * 16384;
          lds_st<f32x4>(wa + o, acc[0]); lds_st<f32x4>(wb + o, acc[1]); lds_st<f32x4>(wa + o + 128, acc[2]); lds_st<f32x4>(wb + o + 128, acc[3]);
        }
        ARR(cR);
        if (g == 0) STAMP(25);
        if (g == 1) STAMP(26);
        if (g == 3) STAMP(30);
        if (g == 2) {
          STAMP(28);
          WAITP(pF, 2);
          STAMP(29);
#pragma unroll
          for (int n = 33; n < 41; ++n) h2_ld(n);
          SB();
        }
        WAIT(cR, 24 + 4 * g);
        if ((w >> 1) == (g & 1)) {
          const u32 r1 = r0 ^ 16, gb = opq(L_G3 + 512 + (l16 & 112) * 2), xa = opq(L_XR + w * 2048 + l16);
          const f32x4 y0 = (lds_ld<f32x4>(r0) + lds_ld<f32x4>(r0 + 4096)) + (lds_ld<f32x4>(r0 + 8192) + lds_ld<f32x4>(r0 + 12288));
          const f32x4 o0 = (g < 2 ? lds_ld<f32x4>(xa) : x1r[0]) + lds_ld<f32x4>(gb) * y0;
          if (g < 3) SB();
          const f32x4 y1 = (lds_ld<f32x4>(r1) + lds_ld<f32x4>(r1 + 4096)) + (lds_ld<f32x4>(r1 + 8192) + lds_ld<f32x4>(r1 + 12288));
          const f32x4 o1 = (g < 2 ? lds_ld<f32x4>(xa + 1024) : x1r[1]) + lds_ld<f32x4>(gb + 16) * y1;
          st16(pack8(o0, o1), ro, (l16 >> 7) * 1024 + (l16 & 112), (T1 + 32 * (g >> 1) + 8 * w) * 1024 + cg * 128);
        }
      }
      STAMP(31);
    }
  } else {
    const srd_t rwo = __builtin_amdgcn_make_buffer_rsrc((void*)P.wo, 0, 512 * 1024, 0x00020000);
    {
      const u32 l = lane_id(), q = l >> 4, tok = l & 15;
      const u32 xo = (T1 - 16 + 4 * w + q) * 1024 + tok * 16;
      const u32 tx = opq(l < 20 ? (T1 - 16) * 1024 + (80 * cg + 20 * w + l) * 128 : OOB);
      const u32 tw = opq(l < 12 ? (48 * r + 12 * w + l) * 1024 + ((3 - ((u32)(size_t)P.wqkv >> 7)) & 7) * 128 : OOB);
      const u32 to = opq(l < 4 ? (16 * r + 4 * w + l) * 1024 + ((3 - ((u32)(size_t)P.wo >> 7)) & 7) * 128 : OOB);
      const u32 tf = opq(l < 4 ? (4 * w + l) * 128 : OOB);
      __builtin_amdgcn_s_barrier();
      asm volatile("" ::: "memory");
      touch(rx, tx, 0); touch(rqkv, tw, 0); touch(rwo, to, 0); touch(ws, tf, WS_FLAG + wg * 2048);
      const u32x2 ep = __builtin_amdgcn_raw_buffer_load_b64(ws, 0, WS_EPOCH, SC1);
      u32x4 xr[5][4];
#pragma unroll
      for (int g = 0; g < 5; ++g) {
        const u32 o = opq(xo + g * 16384);
#pragma unroll
        for (int i = 0; i < 4; ++i) xr[g][i] = __builtin_amdgcn_raw_buffer_load_b128(rx, o + i * 256, 0, 0);
      }
      SB();
      STAMP(1);
      tag = ((u64)rfl(ep.x) | ((u64)rfl(ep.y) << 32)) + 0x40100401ull;
      f32x4 xn[5][4][2];
#pragma unroll
      for (int g = 0; g < 5; ++g) {
        float s = 0.f, ss = 0.f;
#pragma unroll
        for (int i = 0; i < 4; ++i)
#pragma unroll
          for (int j = 0; j < 4; ++j) { s = dot2(xr[g][i][j], 0x3f803f80u, s); ss = dot2(xr[g][i][j], xr[g][i][j], ss); }
        s += dpp<0x128>(s); ss += dpp<0x128>(ss);
        s += dpp<0x124>(s); ss += dpp<0x124>(ss);
        s += dpp<0x122>(s); ss += dpp<0x122>(ss);
        s += dpp<0x121>(s); ss += dpp<0x121>(ss);
        const float m = s * (1.f / 512), rs = __builtin_amdgcn_rsqf(fmaxf(ss * (1.f / 512) - m * m, 0.f) + 1e-5f), nm = -m * rs;
#pragma unroll
        for (int i = 0; i < 4; ++i) {
          xn[g][i][0] = unp4(xr[g][i].x, xr[g][i].y) * rs + nm; xn[g][i][1] = unp4(xr[g][i].z, xr[g][i].w) * rs + nm;
          if (g < 3) asm volatile("" : "+v"(xn[g][i][0]), "+v"(xn[g][i][1]));
        }
        if (g == 2) STAMP(2);
        if (g == 2) break;
      }
      u32 ha[4];
#pragma unroll
      for (int i = 0; i < 4; ++i) ha[i] = opq(((16 * i + tok - 8 * xcd) & 63) * 256 + (((4 * w + q) ^ (tok & 7)) << 4));
      const u32 sba = opq(L_SB + tok * 32);
      __builtin_amdgcn_s_setprio(3);
      f32x4 dummy, mv = sy.gather_mod<false>(ws, 256 * w + 4 * l, tag, 0, dummy);
      if (w >= 2) {
#pragma unroll
        for (int j = 0; j < 4; ++j) mv[j] = rbf(1.f + mv[j]);
      }
      lds_st<f32x4>(L_SB + w * 1024 + l * 16, mv);
      ARR(cM2); WAIT(cM2, 4);
      STAMP(3);
      f32x4 sh[4][2], sl[4][2];
#pragma unroll
      for (int i = 0; i < 4; ++i)
#pragma unroll
        for (int e = 0; e < 2; ++e) { sh[i][e] = lds_ld<f32x4>(sba + i * 512 + e * 16); sl[i][e] = lds_ld<f32x4>(sba + 2048 + i * 512 + e * 16); }
      SB();
#pragma unroll
      for (int i = 0; i < 4; ++i)
#pragma unroll
        for (int g = 0; g < 3; ++g) lds_st<u32x4>(L_H + g * 16384 + ha[i], pack8(xn[g][i][0] * sl[i][0] + sh[i][0], xn[g][i][1] * sl[i][1] + sh[i][1]));
      ARR(cH);
      __builtin_amdgcn_s_setprio(0);
      STAMP(4);
#pragma unroll
      for (int g = 3; g < 5; ++g) {
        float s = 0.f, ss = 0.f;
#pragma unroll
        for (int i = 0; i < 4; ++i)
#pragma unroll
          for (int j = 0; j < 4; ++j) { s = dot2(xr[g][i][j], 0x3f803f80u, s); ss = dot2(xr[g][i][j], xr[g][i][j], ss); }
        s += dpp<0x128>(s); ss += dpp<0x128>(ss);
        s += dpp<0x124>(s); ss += dpp<0x124>(ss);
        s += dpp<0x122>(s); ss += dpp<0x122>(ss);
        s += dpp<0x121>(s); ss += dpp<0x121>(ss);
        const float m = s * (1.f / 512), rs = __builtin_amdgcn_rsqf(fmaxf(ss * (1.f / 512) - m * m, 0.f) + 1e-5f), nm = -m * rs;
#pragma unroll
        for (int i = 0; i < 4; ++i) {
          const f32x4 n0 = unp4(xr[g][i].x, xr[g][i].y) * rs + nm, n1 = unp4(xr[g][i].z, xr[g][i].w) * rs + nm;
          lds_st<u32x4>(L_H + 49152 + (g - 3) * 16384 + ha[i], pack8(n0 * sl[i][0] + sh[i][0], n1 * sl[i][1] + sh[i][1]));
        }
      }
      ARR(cH + 1);
      STAMP(5);
    }
    u32x4 W[48];
    {
      const u32 l = lane_id(), q = l >> 4, tok = l & 15, jj = (l >> 2) & 3;
      const u32 ve = opq((l >> 3) * 1024 + cg * 128 + ((l & 7) ^ ((l >> 3) & 2)) * 16), vo = opq((ve ^ 64) + 7168);
      auto wo_issue = [&](int i) {
        const u32 so = (4 * w + ((i + xcd) & 3)) * 32768;
        dma4(rwo, L_S + i * 16384 + w * 4096, ve, vo, so, so + 14336);
      };
      WAIT(cWc, 4);
      STAMP(6);
      wo_issue(3);
      if (w == 0) gflag(ws, l == 0 ? WS_HELLO + wg * 8 : OOB, tag);
      f32x4 gv4, mv = sy.gather_mod<true>(ws, 1536 + 256 * w + 4 * l, tag, q * 12288 + (((w & 1) ? 2560 : 1024) + 64 * cg + 4 * tok) * 4, gv4);
      WAIT(cWc, 8); wo_issue(0);
      if (w >= 2) {
#pragma unroll
        for (int j = 0; j < 4; ++j) mv[j] = rbf(1.f + mv[j]);
      }
      lds_st<f32x4>(L_G3 + (w & 1) * 512 + tok * 16, gv4);
      WAIT(cH + 1, 4);
      const u32 k = (256 * w + 4 * l) & 511;
      lds_st<u32x2>(L_SB + (w < 2 ? 1024 : 0) + ((k - 64 * xcd) & 511) * 2, pack4(mv));
      if (w >= 2 && (k >> 6) == cg) lds_st<f32x4>(L_G3 + 256 + (k & 63) * 4, mv);
      ARR(cM3);
      WAIT(cWc, 12); wo_issue(1); wo_issue(2);
      STAMP(7);
      f32x4 uv[3] = {};
      const srd_t rg = __builtin_amdgcn_make_buffer_rsrc((void*)P.wg, 0, 512 * 2752, 0x00020000);
      const srd_t ru = __builtin_amdgcn_make_buffer_rsrc((void*)P.wu, 0, 512 * 2752, 0x00020000);
      u32 gv[4];
      {
        const u32 row = 8 * w + q, o = fg == 15 ? OOB : (2752 * row + 192 * fg) & ~127u, sl = tok ^ (((row ^ fg) & 1) | (row & 2) | ((row >> 1) & 4));
        gv[0] = opq(o + (sl << 4)); gv[1] = opq(gv[0] + 11008 - 1024); gv[2] = opq(o + ((sl ^ 8) << 4)); gv[3] = opq(gv[2] + 11008 - 1024);
      }
      constexpr int slot[16] = {0, 1, 2, 0, 1, 3, 2, 0, 1, 3, 2, 0, 1, 3, 2, 4};
      auto issue = [&](int ks) {
        const u32 so = ((ks + 2 * xcd) & 15) * (32 * 2752), d = L_H + slot[ks] * 16384 + w * 2048;
        dma2(rg, d, gv[0], gv[1], so); dma2(ru, d + 8192, gv[2], gv[3], so);
      };
      u32 ga[3];
      {
        const u32 m = (l >> 1) & 1, pr = (jj ^ fg) & 1, gx = pr | (jj & 2) | ((q & 1) << 2) | (m << 3);
#pragma unroll
        for (int b = 0; b < 3; ++b) ga[b] = opq(L_H + m * 8192 + (8 * q + jj) * 256 + (((3 * w + b + 4 * pr) ^ gx) << 4) + (l & 1) * 8);
      }
      const u32 sba = opq(L_SB + (l & 1) * 1024 + q * 16);
      auto rd = [&](int ks) {
        const u32x4 sv = lds_ld<u32x4>(sba + ks * 64);
#pragma unroll
        for (int b = 0; b < 3; ++b) W[ks * 3 + b] = tr_frag(ga[b] + slot[ks] * 16384, 1024);
        SB();
#pragma unroll
        for (int b = 0; b < 3; ++b) uv[b] = mfma32(W[ks * 3 + b], sv, uv[b]);
        SB();
      };
      auto sync = [&](int ks) { ARR(cG); WAIT(cG, 4 * ks + 4); };
      WAIT(cA, 4); issue(0); issue(1); issue(2);
      STAMP(8);
      vmwait<12>(); ARRP(pWo);
      WAIT(cM3, 4);
      vmwait<8>(); sync(0); rd(0);
      STAMP(9);
      vmwait<4>(); sync(1); issue(3); rd(1);
      vmwait<4>(); sync(2); issue(4); rd(2);
      WAIT(cA + 1, 4); issue(5);
      STAMP(10);
      if (wg == 255 && w == 0) {
#pragma unroll 1
        for (int i = 0; i < 4; ++i) sy.poll(ws, WS_HELLO + i * 512 + l * 8, tag);
        gflag(ws, l == 0 ? WS_EPOCH : OOB, tag);
      }
#pragma unroll
      for (int ks = 3; ks < 12; ++ks) { vmwait<8>(); sync(ks); issue(ks + 3); rd(ks); }
      issue(15);
      STAMP(13);
      vmwait<12>(); sync(12); rd(12);
      if (w == 0) sy.poll(ws, WS_FLAG + wg * 2048 + 4 * 128 + (lane_id() & 15) * 8, tag);
      vmwait<8>(); sync(13);
      STAMP(14);
      x1_issue(0); x1_issue(1);
      if (w == 0) ARR(cXs);
      rd(13);
      vmwait<16>(); sync(14); rd(14); rd(15);
      const u32 l2 = lane_id();
      if ((l2 & 15) < 2) {
#pragma unroll
        for (int b = 0; b < 3; ++b) lds_st<f32x4>(L_UV + w * 384 + b * 128 + (l2 & 1) * 64 + (l2 >> 4) * 16, (l2 & 33) == 1 ? uv[b] * -LOG2E : uv[b]);
      }
    }
    {
      const u32 l = lane_id(), q = l >> 4, tok = l & 15;
      u32 xa[4], db = opq(L_GD + 24576 + w * 6144 + l * 16), cs = sy.cb;
#pragma unroll
      for (int k = 0; k < 4; ++k) xa[k] = opq(L_X0 + tok * 1024 + (((4 * k + q) ^ tok) << 4));
      auto xf = [&](int t, int ks) { return lds_ld<u32x4>(xa[ks & 3] + (ks >> 2) * 256 + t * 16384); };
      f32x4 acc[2][3] = {};
      u32x4 a[4];
      vmwait<8>(); ARR(cX);
      STAMP(15);
      __builtin_amdgcn_s_setprio(2);
      WAIT(cX, 4);
      STAMP(16);
#pragma unroll
      for (int n = 0; n < 4; ++n) a[n] = xf(0, n);
#pragma unroll 1
      for (u32 s = 0; s < 4; ++s) {
        u32 pk;
        SB();
#pragma unroll
        for (int n = 0; n < 32; ++n) {
          const int t = n >> 4, ks = n & 15;
          if (n == 28) {
            if (s < 3 && rfl(pk) < 4) sy.wait<cX + 1>(cs, 4);
#pragma unroll
            for (int k = 0; k < 4; ++k) xa[k] ^= (L_X0 ^ L_X1);
            SB();
          }
#pragma unroll
          for (int b = 0; b < 3; ++b) acc[t][b] = mfma32(W[ks * 3 + b], a[n & 3], ks == 0 ? z4 : acc[t][b]);
          a[n & 3] = xf(((n + 4) >> 4) & 1, (n + 4) & 15);
          if (n >= 1 && n < 4) lds_st<f32x4>(db + (3 + n - 1) * 1024, acc[1][n - 1]);
          if (n == 4) { sy.add<cB - 1>(cs); db += (s & 1) ? 24576 : -24576; }
          if (n == 12) pk = *(volatile LAS u32*)(sy.pb + 4 * pDf);
          if (n == 16) {
            if (s == 0) { vmwait<0>(); ARR(cX + 1); }
            const u32 tg = (s >> 1) << (s & 1);
            if (rfl(pk) < tg) WAITP(pDf, tg);
          }
          if (n >= 17 && n < 20) lds_st<f32x4>(db + (n - 17) * 1024, acc[0][n - 17]);
          if (n == 24) pk = *(volatile LAS u32*)(cs + 4 * (cX + 1));
          SB();
        }
        cs += 4;
      }
      STAMP(20);
      const u32 l2 = lane_id(), ti = l2 >> 5, bq = (l2 >> 4) & 1, pc = 12 * fg + 3 * w, vb = ti * (4 * H2_RB) + (l2 & 15) * 16;
      WAIT(cS + 1, 4);
      const f32x2 st = lds_ld<f32x2>(L_RS + 768 + ti * 128 + (l2 & 15) * 8);
      const u32 uvb = opq(L_UV + w * 384 + bq * 16);
      u32x2 h[3];
#pragma unroll
      for (int b = 0; b < 3; ++b) {
        f32x4 g, u;
#pragma unroll
        for (int k = 0; k < 4; ++k) { auto t = __builtin_amdgcn_permlane32_swap(uf(acc[0][b][k]), uf(acc[1][b][k]), false, false); g[k] = fu(t[0]); u[k] = fu(t[1]); }
        h[b] = glu(g, u, uvb + b * 128, st[0], st[1]);
      }
      const u32 so = WS_H2 + ((T3 >> 4) + 3) * H2_RB;
      {
        auto ex = __builtin_amdgcn_permlane16_swap(h[0].x, h[1].x, false, false);
        auto ey = __builtin_amdgcn_permlane16_swap(h[0].y, h[1].y, false, false);
        st16_sc1(u32x4{ex[0], ey[0], ex[1], ey[1]}, ws, pc + bq < 172 ? vb + (pc + bq) * 256 : OOB, so);
        ex = __builtin_amdgcn_permlane16_swap(h[2].x, h[2].x, false, false);
        ey = __builtin_amdgcn_permlane16_swap(h[2].y, h[2].y, false, false);
        st16_sc1(u32x4{ex[0], ey[0], ex[1], ey[1]}, ws, (bq == 0 && pc + 2 < 172) ? vb + (pc + 2) * 256 : OOB, so);
      }
      __builtin_amdgcn_s_setprio(0);
      STAMP(21);
      const u32 pv = WS_FLAG + wg * 2048 + (9 + 2 * w) * 128 + (l2 < (w == 1 ? 5 : 4) ? l2 : 0) * 8;
      const bool ok = sy.poll_once(ws, pv, tag);
      publish_h2(3, LAST(cPh + 3));
      STAMP(22);
      if (!ok) sy.poll(ws, pv, tag);
      ARRP(pF);
      sy.poll(ws, pv + 64, tag);
      ARRP(pF);
      STAMP(23);
    }
  }
}

extern "C" void kernel_launch(void* const* d_in, const int* in_sizes, int n_in, void* d_out, int out_size, void* d_ws, size_t ws_size,
                              hipStream_t stream) {
  Params p;
  p.x = d_in[0]; p.wqkv = d_in[1]; p.wo = d_in[2]; p.wg = d_in[4]; p.wu = d_in[5]; p.wd = d_in[6]; p.c = d_in[7]; p.wmod = d_in[8];
  p.out = d_out; p.ws = d_ws;
  adaln_block<<<256, 512, 0, stream>>>(p);
}
```

```cpp
#include <hip/hip_runtime.h>

#define DI __device__ __forceinline__
#define LAS __attribute__((address_space(3)))
typedef unsigned u32;
typedef unsigned long long u64;
typedef u32 u32x2 __attribute__((ext_vector_type(2)));
typedef u32 u32x4 __attribute__((ext_vector_type(4)));
typedef float f32x2 __attribute__((ext_vector_type(2)));
typedef float f32x4 __attribute__((ext_vector_type(4)));
typedef short s16x4 __attribute__((ext_vector_type(4)));
typedef __bf16 bf16x2 __attribute__((ext_vector_type(2)));
typedef __bf16 bf16x8 __attribute__((ext_vector_type(8)));
typedef __amdgpu_buffer_rsrc_t srd_t;

constexpr u32 WS_EPOCH = 0, WS_HELLO = 4096, WS_MODP = 8192, WS_FLAG = 65536, WS_STAT = 0x100000, WS_O = 0x200000,
              WS_X1 = 0x400000, WS_H2 = 0x600000, WS_STAMP = 0xC00000, WS_WG = 0xD00000, WS_WD = 0x1000000, WS_END = 0x1200000,
              WS_FG = 0xA0000  , WS_FD = 0xA4000  ;
constexpr u32 H2_RB = 172 * 256;
constexpr u32 FS = 17 * 128;
constexpr u32 OOB = 0x80000000u;
constexpr int SC1 = 16;
constexpr u32 L_H = 0, L_S = 81920, L_XR = 147456, L_JUNK = 157440, L_CNT = 157696, L_SB = 157952, L_RS = 162048, L_G3 = 163072;
constexpr u32 L_Q = L_S - 24576;
constexpr u32 L_UV = L_SB + 2048, L_X0 = L_S + 32768, L_X1 = L_H, L_X2 = L_S, L_GD = L_H + 32768;
enum { cW = 0, cM1, cM2, cM3, cR, cPa, cA, cXs  , cS = 9, cL = 11  , cI4 = 14, cPx = 15, cH = 17, cB = 19, cX = 22, cPh = 27, cWc = 31  , cF = 36  , cGu = 38, cXi = 39, cXa = 35, cD = 52  ,
       pWo = 0, pDf = 4, pB = 8 };
#define AB 0
#define AB_GU (AB & 1)
#define AB_MR (AB & 2)
#define AB_C3 (AB & 4)
#define AB_X34 (AB & 8)
#define EW1 8
#define EW2 16
#define PAUSEAT 31
#define PSLEEP "2"
constexpr u32 CODE_BYTES = 40960;
#define SPIN (1 << 16)
constexpr float LOG2E = 1.4426950408889634f;

#define STAMP(k) do {} while (0)
#define SB() __builtin_amdgcn_sched_barrier(0)

DI u32 rfl(u32 x) { return __builtin_amdgcn_readfirstlane(x); }
DI u32 opq(u32 x) { asm volatile("" : "+v"(x)); return x; }
DI u32 lane_id() { u32 l; asm volatile("v_mbcnt_lo_u32_b32 %0, -1, 0\n\tv_mbcnt_hi_u32_b32 %0, -1, %0" : "=v"(l)); return l; }
DI float fu(u32 x) { return __builtin_bit_cast(float, x); }
DI u32 uf(float x) { return __builtin_bit_cast(u32, x); }
DI float bflo(u32 v) { return fu(v << 16); }
DI float bfhi(u32 v) { return fu(v & 0xffff0000u); }
DI u32 cvtpk(float lo, float hi) { f32x2 v = {lo, hi}; return __builtin_bit_cast(u32, __builtin_convertvector(v, bf16x2)); }
DI u32x2 pack4(f32x4 v) { return u32x2{cvtpk(v[0], v[1]), cvtpk(v[2], v[3])}; }
DI u32x4 pack8(f32x4 a, f32x4 b) { return u32x4{cvtpk(a[0], a[1]), cvtpk(a[2], a[3]), cvtpk(b[0], b[1]), cvtpk(b[2], b[3])}; }
DI f32x4 unp4(u32 a, u32 b) { return f32x4{bflo(a), bfhi(a), bflo(b), bfhi(b)}; }
DI float rbf(float x) { return bflo(cvtpk(x, x)); }
DI float dot2(u32 a, u32 b, float c) { return __builtin_amdgcn_fdot2_f32_bf16(__builtin_bit_cast(bf16x2, a), __builtin_bit_cast(bf16x2, b), c, false); }
DI f32x4 mfma32(u32x4 a, u32x4 b, f32x4 c) {
  return __builtin_amdgcn_mfma_f32_16x16x32_bf16(__builtin_bit_cast(bf16x8, a), __builtin_bit_cast(bf16x8, b), c, 0, 0, 0);
}
DI f32x4 mfma16(u32x2 a, u32x2 b, f32x4 c) {
  return __builtin_amdgcn_mfma_f32_16x16x16bf16_1k(__builtin_bit_cast(s16x4, a), __builtin_bit_cast(s16x4, b), c, 0, 0, 0);
}
DI void mma(f32x4& c, u32x4 a, u32x4 b) { asm volatile("v_mfma_f32_16x16x32_bf16 %0, %1, %2, %0" : "+v"(c) : "v"(a), "v"(b)); }
DI void mma0(f32x4& c, u32x4 a, u32x4 b) { asm volatile("v_mfma_f32_16x16x32_bf16 %0, %1, %2, 0" : "=&v"(c) : "v"(a), "v"(b)); }
DI void mma16(f32x4& c, u32x2 a, u32x2 b) { asm volatile("v_mfma_f32_16x16x16_bf16 %0, %1, %2, %0" : "+v"(c) : "v"(a), "v"(b)); }
DI void mmaz(bool first, f32x4& c, u32x4 a, u32x4 b) { if (first) mma0(c, a, b); else mma(c, a, b); }
DI float fmx(float a, float b) { return __builtin_elementwise_maximum(a, b); }
template <int C> DI float dpp(float x) { return fu(__builtin_amdgcn_update_dpp(0, (int)uf(x), C, 0xf, 0xf, true)); }
DI float adds(float a, float b) { float r; asm("v_add_f32 %0, %1, %2" : "=v"(r) : "v"(a), "v"(b)); return r; }
DI float sum16(float x) { auto r = __builtin_amdgcn_permlane16_swap(uf(x), uf(x), false, false); return fu(r[0]) + fu(r[1]); }
DI float sum32(float x) { auto r = __builtin_amdgcn_permlane32_swap(uf(x), uf(x), false, false); return fu(r[0]) + fu(r[1]); }
DI float max16(float x) { auto r = __builtin_amdgcn_permlane16_swap(uf(x), uf(x), false, false); return fmx(fu(r[0]), fu(r[1])); }
DI float max32(float x) { auto r = __builtin_amdgcn_permlane32_swap(uf(x), uf(x), false, false); return fmx(fu(r[0]), fu(r[1])); }

template <class T> DI T lds_ld(u32 a) { return *(LAS T*)a; }
template <class T> DI void lds_st(u32 a, T v) { *(LAS T*)a = v; }
DI u32x4 tr_frag(u32 a, u32 hstep) {
  s16x4 lo = __builtin_amdgcn_ds_read_tr16_b64_v4i16((LAS s16x4*)a);
  s16x4 hi = __builtin_amdgcn_ds_read_tr16_b64_v4i16((LAS s16x4*)(a + hstep));
  u32x2 l = __builtin_bit_cast(u32x2, lo), h = __builtin_bit_cast(u32x2, hi);
  return u32x4{l.x, l.y, h.x, h.y};
}
DI void dma2(srd_t r, u32 lds, u32 v0, u32 v1, u32 so) {
  asm volatile("s_mov_b32 m0, %0\n\ts_nop 0\n\tbuffer_load_dwordx4 %1, %3, %4 offen lds\n\tbuffer_load_dwordx4 %2, %3, %4 offen offset:1024 lds"
               ::"s"(lds), "v"(v0), "v"(v1), "s"(r), "s"(so) : "memory");
}
DI void dma4(srd_t r, u32 lds, u32 v0, u32 v1, u32 s0, u32 s1) {
  asm volatile("s_mov_b32 m0, %0\n\ts_nop 0\n\tbuffer_load_dwordx4 %1, %3, %4 offen lds\n\tbuffer_load_dwordx4 %2, %3, %4 offen offset:1024 lds\n\t"
               "buffer_load_dwordx4 %1, %3, %5 offen offset:2048 lds\n\tbuffer_load_dwordx4 %2, %3, %5 offen offset:3072 lds"
               ::"s"(lds), "v"(v0), "v"(v1), "s"(r), "s"(s0), "s"(s1) : "memory");
}
DI void dma1_sc1(srd_t r, u32 lds, u32 v0, u32 so) {
  asm volatile("s_mov_b32 m0, %0\n\ts_nop 0\n\tbuffer_load_dwordx4 %1, %2, %3 offen sc1 lds" ::"s"(lds), "v"(v0), "s"(r), "s"(so) : "memory");
}
DI void dma4_sc1(srd_t r, u32 lds, u32 v0, u32 so) {
  asm volatile("s_mov_b32 m0, %0\n\ts_nop 0\n\tbuffer_load_dwordx4 %1, %5, %6 offen sc1 lds\n\tbuffer_load_dwordx4 %2, %5, %6 offen offset:1024 sc1 lds\n\t"
               "buffer_load_dwordx4 %3, %5, %6 offen offset:2048 sc1 lds\n\tbuffer_load_dwordx4 %4, %5, %6 offen offset:3072 sc1 lds"
               ::"s"(lds), "v"(v0), "v"(v0 ^ 16), "v"(v0 ^ 32), "v"(v0 ^ 48), "s"(r), "s"(so) : "memory");
}
DI void touch(srd_t r, u32 voff, u32 soff) {
  asm volatile("s_mov_b32 m0, %0\n\ts_nop 0\n\tbuffer_load_dword %1, %2, %3 offen sc1 lds" ::"s"(L_JUNK), "v"(voff), "s"(r), "s"(soff) : "memory");
}
DI void retouch(srd_t r, u32 voff, u32 soff) { touch(r, voff, soff); }
template <int N> DI void vmwait() { asm volatile("s_waitcnt vmcnt(%0)" ::"i"(N) : "memory"); }
DI void lgwait() { asm volatile("s_waitcnt lgkmcnt(0)" ::: "memory"); }
DI void st16_sc1(u32x4 v, srd_t r, u32 voff, u32 soff) {
  asm volatile("buffer_store_dwordx4 %0, %1, %2, %3 offen sc1\n\ts_nop 1" ::"v"(v), "v"(voff), "s"(r), "s"(soff) : "memory");
}
DI void st16(u32x4 v, srd_t r, u32 voff, u32 soff) {
  asm volatile("buffer_store_dwordx4 %0, %1, %2, %3 offen\n\ts_nop 1" ::"v"(v), "v"(voff), "s"(r), "s"(soff) : "memory");
}
DI u32x4 ld16h(srd_t r, u32 voff, u32 soff, int imm) {
  u32x4 v; asm volatile("buffer_load_dwordx4 %0, %1, %2, %3 offen offset:%4 sc1" : "=v"(v) : "v"(voff), "s"(r), "s"(soff), "i"(imm) : "memory"); return v;
}
DI u64 ld8h(srd_t r, u32 voff) { u64 v; asm volatile("buffer_load_dwordx2 %0, %1, %2, 0 offen sc1" : "=v"(v) : "v"(voff), "s"(r) : "memory"); return v; }
DI void at8h(u64& v, srd_t r, u32 voff, u64 m) {
  asm volatile("v_mov_b64 %0, 0\n\ts_mov_b64 exec, %3\n\tbuffer_atomic_or_x2 %0, %1, %2, 0 offen sc0 sc1\n\ts_mov_b64 exec, -1" : "+v"(v) : "v"(voff), "s"(r), "s"(m) : "memory");
}
DI bool all_eq(u64 v, u64 tag, u64 m = ~0ull) { u64 c; asm volatile("v_cmp_ne_u64 %0, %2, %1" : "=s"(c) : "v"(v), "s"(tag) : "memory"); return (c & m) == 0; }
DI u32 sel(bool c, u32 v) { v = opq(v); return c ? v : OOB; }
DI void gflag(srd_t r, u32 voff, u64 tag) { __builtin_amdgcn_raw_buffer_store_b64(u32x2{(u32)tag, (u32)(tag >> 32)}, r, voff, 0, SC1); }

struct Sync {
  u32 cb, pb, one; int n;
  template <int I> DI void add(u32 a) {
    asm volatile("s_mov_b64 exec, 1\n\tds_add_u32 %0, %1 offset:%2\n\ts_mov_b64 exec, -1" ::"v"(a), "v"(one), "i"(4 * I) : "memory");
  }
  template <int I> DI u32 add_rtn(u32 a) {
    u32 v, s;
    asm volatile("s_mov_b64 exec, 1\n\tds_add_rtn_u32 %0, %2, %3 offset:%4\n\ts_mov_b64 exec, -1\n\ts_waitcnt lgkmcnt(0)\n\tv_readfirstlane_b32 %1, %0"
                 : "=&v"(v), "=s"(s) : "v"(a), "v"(one), "i"(4 * I) : "memory");
    return s;
  }
  template <int I> DI u32 add_go(u32 a) {
    u32 v;
    asm volatile("s_mov_b64 exec, 1\n\tds_add_rtn_u32 %0, %1, %2 offset:%3\n\ts_mov_b64 exec, -1" : "=v"(v) : "v"(a), "v"(one), "i"(4 * I) : "memory");
    return v;
  }
  DI u32 add_get(u32 v) { u32 s; asm volatile("s_waitcnt lgkmcnt(0)\n\tv_readfirstlane_b32 %0, %1" : "=s"(s) : "v"(v) : "memory"); return s == 3 ? 0u : OOB; }
  template <int I> DI void wait(u32 a, u32 target) {
    u32 v, s;
    asm volatile("1:\n\tds_read_b32 %0, %3 offset:%5\n\ts_sub_i32 %2, %2, 1\n\ts_waitcnt lgkmcnt(0)\n\tv_readfirstlane_b32 %1, %0\n\ts_cmp_lt_u32 %1, %4\n\t"
                 "s_cselect_b32 %1, %2, 0\n\ts_cmp_gt_i32 %1, 0\n\ts_cbranch_scc1 1b"
                 : "=&v"(v), "=&s"(s), "+s"(n) : "v"(a), "s"(target), "i"(4 * I) : "memory", "scc");
  }
  template <int I> DI void sub(u32 a) {
    asm volatile("s_mov_b64 exec, 1\n\tds_sub_u32 %0, %1 offset:%2\n\ts_mov_b64 exec, -1" ::"v"(a), "v"(one), "i"(4 * I) : "memory");
  }
  template <int I> DI void waitz(u32 a) {
    u32 v, s;
    asm volatile("1:\n\tds_read_b32 %0, %3 offset:%4\n\ts_sub_i32 %2, %2, 1\n\ts_waitcnt lgkmcnt(0)\n\tv_readfirstlane_b32 %1, %0\n\ts_cmp_lg_u32 %1, 0\n\t"
                 "s_cselect_b32 %1, %2, 0\n\ts_cmp_gt_i32 %1, 0\n\ts_cbranch_scc1 1b"
                 : "=&v"(v), "=&s"(s), "+s"(n) : "v"(a), "i"(4 * I) : "memory", "scc");
  }
  DI void poll(srd_t r, u32 voff, u64 tag, u64 m) {
    u64 v; u32 s;
    asm volatile("s_mov_b64 exec, %6\n1:\n\tv_mov_b64 %0, 0\n\ts_sleep " PSLEEP "\n\tbuffer_atomic_or_x2 %0, %3, %4, 0 offen sc0 sc1\n\ts_sub_i32 %1, %1, 8\n\ts_waitcnt vmcnt(0)\n\t"
                 "v_cmp_ne_u64 vcc, %5, %0\n\ts_cmp_lg_u64 vcc, 0\n\ts_cselect_b32 %2, %1, 0\n\ts_cmp_gt_i32 %2, 0\n\ts_cbranch_scc1 1b\n\ts_mov_b64 exec, -1"
                 : "=&v"(v), "+s"(n), "=&s"(s) : "v"(voff), "s"(r), "s"(tag), "s"(m) : "memory", "scc", "vcc");
  }
  template <int N> DI void mod_ld(u32x4 (&p)[N], srd_t ws, u32 co, u32 xo) {
#pragma unroll
    for (int k = 0; k < 4; ++k) p[k] = ld16h(ws, co, WS_MODP + k * 12288, 0);
    if (N == 5) p[N - 1] = ld16h(ws, xo, WS_MODP, 0);
  }
  template <int N> DI f32x4 mod_get(u32x4 (&p)[N], srd_t ws, u32 co, u32 xo, u64 tag) {
    const u32 t0 = tag & 1023, t1 = (tag >> 10) & 1023, t2 = (tag >> 20) & 1023, t3 = (tag >> 30) & 1023;
    while (true) {
      u32 bad = 0;
#pragma unroll
      for (int k = 0; k < N; ++k) { asm volatile("" : "+v"(p[k])); bad |= (p[k].x ^ t0) | (p[k].y ^ t1) | (p[k].z ^ t2) | (p[k].w ^ t3); }
      n -= 8;
      if (__builtin_expect(__builtin_amdgcn_ballot_w64((bad & 1023) != 0) == 0 || n <= 0, 1)) break;
      asm volatile("buffer_load_dwordx4 %0, %4, %5, %6 offen sc1\n\tbuffer_load_dwordx4 %1, %4, %5, %7 offen sc1\n\tbuffer_load_dwordx4 %2, %4, %5, %8 offen sc1\n\t"
                   "buffer_load_dwordx4 %3, %4, %5, %9 offen sc1\n\ts_waitcnt vmcnt(0)" : "=&v"(p[0]), "=&v"(p[1]), "=&v"(p[2]), "=&v"(p[3])
                   : "v"(co), "s"(ws), "s"(WS_MODP), "s"(WS_MODP + 12288), "s"(WS_MODP + 24576), "s"(WS_MODP + 36864) : "memory");
      if (N == 5) asm volatile("buffer_load_dwordx4 %0, %1, %2, %3 offen sc1\n\ts_waitcnt vmcnt(0)" : "=&v"(p[N - 1]) : "v"(xo), "s"(ws), "s"(WS_MODP) : "memory");
    }
    f32x4 r;
#pragma unroll
    for (int j = 0; j < 4; ++j) r[j] = rbf(adds(adds(fu(p[0][j] & ~1023u), fu(p[1][j] & ~1023u)), adds(fu(p[2][j] & ~1023u), fu(p[3][j] & ~1023u))));
    return r;
  }
};
struct F4 { float a, b, c, d; };
DI F4 glu1(f32x4 g, float rn) {
  F4 e;
  asm volatile("v_mul_f32 %0, %8, %4\n\tv_mul_f32 %1, %8, %5\n\tv_mul_f32 %2, %8, %6\n\tv_mul_f32 %3, %8, %7\n\t"
               "v_exp_f32 %0, %0\n\tv_exp_f32 %1, %1\n\tv_exp_f32 %2, %2\n\tv_exp_f32 %3, %3\n\t"
               "v_add_f32 %0, 1.0, %0\n\tv_add_f32 %1, 1.0, %1\n\tv_add_f32 %2, 1.0, %2\n\tv_add_f32 %3, 1.0, %3"
               : "=&v"(e.a), "=&v"(e.b), "=&v"(e.c), "=&v"(e.d) : "v"(g[0]), "v"(g[1]), "v"(g[2]), "v"(g[3]), "v"(rn));
  return e;
}
DI u32x2 glu2(F4 e, f32x4 u, float rs) {
  F4 t; u32 x, y;
  asm volatile("v_rcp_f32 %6, %6\n\tv_rcp_f32 %7, %7\n\tv_rcp_f32 %8, %8\n\tv_rcp_f32 %9, %9\n\t"
               "v_mul_f32 %2, %14, %10\n\tv_mul_f32 %3, %14, %11\n\tv_mul_f32 %4, %14, %12\n\tv_mul_f32 %5, %14, %13\n\t"
               "v_mul_f32 %2, %2, %6\n\tv_mul_f32 %3, %3, %7\n\tv_mul_f32 %4, %4, %8\n\tv_mul_f32 %5, %5, %9\n\t"
               "v_cvt_pk_bf16_f32 %0, %2, %3\n\tv_cvt_pk_bf16_f32 %1, %4, %5\n\ts_nop 1"
               : "=&v"(x), "=&v"(y), "=&v"(t.a), "=&v"(t.b), "=&v"(t.c), "=&v"(t.d), "+v"(e.a), "+v"(e.b), "+v"(e.c), "+v"(e.d)
               : "v"(u[0]), "v"(u[1]), "v"(u[2]), "v"(u[3]), "v"(rs));
  return u32x2{x, y};
}
DI f32x4 resid4(f32x4 a, f32x4 b, f32x4 c, f32x4 d, f32x4 x, f32x4 g) {
  F4 p, t;
  asm("v_add_f32 %0, %8, %12\n\tv_add_f32 %1, %9, %13\n\tv_add_f32 %2, %10, %14\n\tv_add_f32 %3, %11, %15\n\t"
      "v_add_f32 %4, %16, %20\n\tv_add_f32 %5, %17, %21\n\tv_add_f32 %6, %18, %22\n\tv_add_f32 %7, %19, %23\n\t"
      "v_add_f32 %0, %0, %4\n\tv_add_f32 %1, %1, %5\n\tv_add_f32 %2, %2, %6\n\tv_add_f32 %3, %3, %7"
      : "=&v"(p.a), "=&v"(p.b), "=&v"(p.c), "=&v"(p.d), "=&v"(t.a), "=&v"(t.b), "=&v"(t.c), "=&v"(t.d)
      : "v"(a[0]), "v"(a[1]), "v"(a[2]), "v"(a[3]), "v"(b[0]), "v"(b[1]), "v"(b[2]), "v"(b[3]), "v"(c[0]), "v"(c[1]), "v"(c[2]), "v"(c[3]),
        "v"(d[0]), "v"(d[1]), "v"(d[2]), "v"(d[3]));
  asm("v_fma_f32 %0, %8, %4, %12\n\tv_fma_f32 %1, %9, %5, %13\n\tv_fma_f32 %2, %10, %6, %14\n\tv_fma_f32 %3, %11, %7, %15"
      : "=&v"(t.a), "=&v"(t.b), "=&v"(t.c), "=&v"(t.d)
      : "v"(p.a), "v"(p.b), "v"(p.c), "v"(p.d), "v"(g[0]), "v"(g[1]), "v"(g[2]), "v"(g[3]), "v"(x[0]), "v"(x[1]), "v"(x[2]), "v"(x[3]));
  return f32x4{t.a, t.b, t.c, t.d};
}
DI u32x2 scale4(f32x4 u, f32x4 s) {
  F4 t; u32 x, y;
  asm("v_mul_f32 %2, %6, %10\n\tv_mul_f32 %3, %7, %11\n\tv_mul_f32 %4, %8, %12\n\tv_mul_f32 %5, %9, %13\n\tv_cvt_pk_bf16_f32 %0, %2, %3\n\tv_cvt_pk_bf16_f32 %1, %4, %5"
      : "=&v"(x), "=&v"(y), "=&v"(t.a), "=&v"(t.b), "=&v"(t.c), "=&v"(t.d)
      : "v"(u[0]), "v"(u[1]), "v"(u[2]), "v"(u[3]), "v"(s[0]), "v"(s[1]), "v"(s[2]), "v"(s[3]));
  return u32x2{x, y};
}
DI void mom8(f32x4 a, f32x4 b, float& sm, float& sq) {
  float s2, q2;
  asm("v_add_f32 %0, %4, %5\n\tv_mul_f32 %1, %4, %4\n\tv_add_f32 %2, %8, %9\n\tv_mul_f32 %3, %8, %8\n\t"
      "v_add_f32 %0, %0, %6\n\tv_fmac_f32 %1, %5, %5\n\tv_add_f32 %2, %2, %10\n\tv_fmac_f32 %3, %9, %9\n\t"
      "v_add_f32 %0, %0, %7\n\tv_fmac_f32 %1, %6, %6\n\tv_add_f32 %2, %2, %11\n\tv_fmac_f32 %3, %10, %10\n\t"
      "v_fmac_f32 %1, %7, %7\n\tv_fmac_f32 %3, %11, %11\n\tv_add_f32 %0, %0, %2\n\tv_add_f32 %1, %1, %3\n\ts_nop 1"
      : "=&v"(sm), "=&v"(sq), "=&v"(s2), "=&v"(q2)
      : "v"(a[0]), "v"(a[1]), "v"(a[2]), "v"(a[3]), "v"(b[0]), "v"(b[1]), "v"(b[2]), "v"(b[3]));
}
constexpr u64 lane_mask(int kind) {
  u64 m = 0;
  for (int l = 0; l < 64; ++l) {
    const int tok = l & 15, q = l >> 4, par = tok & 1;
    if (kind == 0 ? par : 4 * q + 2 * (kind - 1) + par <= tok) m |= 1ull << l;
  }
  return m;
}
#define ARR(i) sy.add<i>(sy.cb)
#define ARRP(i) sy.add<i>(sy.pb)
#define LAST(i) (sy.add_rtn<i>(sy.cb) == 3)
#define PEEK(i) (*(volatile LAS u32*)(sy.cb + 4 * (i)))
#define CHECK(pk, i, t) do { if (UNLIKELY(rfl(pk) < (t))) WAIT(i, t); } while (0)
#define UNLIKELY(c) __builtin_expect(!!(c), 0)
#define IFLAST(i) (LAST(i) ? 0u : OOB)
#define WAIT(i, t) sy.wait<i>(sy.cb, t)
#define WAITP(i, t) sy.wait<i>(sy.pb, t)

struct Params {
  const void *x, *wqkv, *wo, *wg, *wu, *wd, *c, *wmod;
  void *out, *ws;
};

__global__ void __launch_bounds__(512, 2) adaln_block(Params P) {
  __shared__ __attribute__((aligned(16))) char smem[163840];
  const u32 wave = rfl(threadIdx.x >> 6), w = wave & 3;
  const u32 wg = blockIdx.x, xcd = wg & 7, r = wg >> 3, tq = r >> 3, ci = r & 7, fg = 2 * ci + (xcd & 1);
  const u32 T1 = xcd * 256 + tq * 64, T3 = (xcd >> 1) * 512 + tq * 128;
  asm volatile("" ::"s"((u32)(size_t)(LAS char*)smem) : "memory");
  STAMP(0);
  const u32 cj = tq * 7 + ci, mu = ci == 7 ? 255 : cj * 8 + xcd;
  const u32 l0 = threadIdx.x & 63, kq = mu / 48, lc = mu - kq * 48;
  {
    u64 pc;
    asm volatile("s_getpc_b64 %0" : "=s"(pc));
    touch(__builtin_amdgcn_make_buffer_rsrc((void*)(pc & ~127ull), 0, CODE_BYTES, 0x00020000), l0 * 32768 + (wave * 32 + r) * 128, 0);
    lds_st<u32>(L_CNT + 4 * l0, 0u);
  }
  const u32 k0 = 128 * kq + 32 * w + 4 * (l0 >> 3), cv0 = opq(k0 * 2);
  u32 mv0 = opq(k0 * 6144 + lc * 128 + (l0 & 7) * 16);
  const u32 mvu = opq(lc < 16 ? mv0 : OOB), mvv = opq(mvu + 12288);
  const u32 tx = opq(sel(l0 < 20, (T1 - 16) * 1024 + (80 * ci + 20 * w + l0) * 128)), t4 = opq(sel(l0 < 4, l0 * 128));
  SB();
  const srd_t ws = __builtin_amdgcn_make_buffer_rsrc(P.ws, 0, WS_END, 0x00020000);
  const srd_t rm = __builtin_amdgcn_make_buffer_rsrc((void*)P.wmod, 0, 512 * 6144, 0x00020000);
  u32x4 wm[4];
  u64 cv, ep;
  if (wave < 4) {
    cv = ld8h(__builtin_amdgcn_make_buffer_rsrc((void*)P.c, 0, 1024, 0x00020000), cv0);
    ep = ld8h(ws, WS_EPOCH);
#pragma unroll
    for (int i = 0; i < 4; ++i) wm[i] = ld16h(rm, i < 2 ? mvu : mvv, (i & 1) * 6144, 0);
  }
  SB();
  lgwait();
  __builtin_amdgcn_s_barrier();
  asm volatile("" ::: "memory");
  STAMP(44);
  SB();
  const u32 cs = (3 - ((u32)(size_t)P.wqkv >> 7)) & 7, cg = (ci + cs + 1) & 7;
  const bool wnone = 12 * fg + 3 * w >= 172;
  const srd_t rx = __builtin_amdgcn_make_buffer_rsrc((void*)P.x, 0, 2048 * 1024, 0x00020000);
  const srd_t rqkv = __builtin_amdgcn_make_buffer_rsrc((void*)P.wqkv, 0, 512 * 3072, 0x00020000);
  const f32x4 z4 = {0.f, 0.f, 0.f, 0.f};
  const u32 FL = WS_FLAG + (xcd * 32 + r) * FS;
  Sync sy;
  sy.n = SPIN;
  u64 tag;
  auto qkv_issue = [&](int c, u32 v0, u32 v1) {
    const u32 so = ((c + xcd) & 7) * (64 * 3072), d = L_Q + (c & 3) * 24576 + w * 2048;
#pragma unroll
    for (int s = 0; s < 3; ++s) dma2(rqkv, d + s * 8192, v0, v1, so + s * 1024);
  };
  auto qkv_seg = [&](int c, int s, u32 v0, u32 v1) {
    dma2(rqkv, L_Q + (c & 3) * 24576 + w * 2048 + s * 8192, v0, v1, ((c + xcd) & 7) * (64 * 3072) + s * 1024);
  };
  auto stats_ld = [&](int j, u32 l16) { return ld16h(ws, l16, WS_STAT + ((T3 >> 4) + 4 * (w >> 1) + 2 * j + (w & 1)) * 1024, 0); };
  auto stats = [&](int j, u32x4 p, u32 l16) {
    const u32 tk = ((2 * j + (w & 1)) * 2 + (w >> 1)) * 16 + ((l16 >> 3) & 14) + (l16 >> 9);
    asm volatile("" : "+v"(p));
    auto a = __builtin_amdgcn_permlane32_swap(p.x, p.z, false, false);
    auto b = __builtin_amdgcn_permlane32_swap(p.y, p.w, false, false);
    auto c = __builtin_amdgcn_permlane16_swap(uf(adds(fu(a[0]), fu(a[1]))), uf(adds(fu(b[0]), fu(b[1]))), false, false);
    float t = fu(c[0]) + fu(c[1]);
    t = (t + dpp<0x128>(t)) * (1.f / 512);
    auto d = __builtin_amdgcn_permlane16_swap(uf(t), uf(t), false, false);
    const float ve = fmx(__builtin_fmaf(-fu(d[0]), fu(d[0]), fu(d[1])), 0.f) + 1e-5f, rs = __builtin_amdgcn_rsqf(ve);
    const float z = __builtin_amdgcn_inverse_ballot_w64(0xffff0000ffff0000ull) ? ve * rs : -fu(d[0]);
    const u32 hi = cvtpk(z, 0.f);
    lds_st<u32x2>(L_UV + tk * 16 + ((l16 >> 5) & 8), u32x2{cvtpk(z, z - bflo(hi)), hi});
    lds_st<float>(L_RS + tk * 4, rs);
  };
  auto h2_flagoff = [&]() { const u32 l = lane_id(); return opq(sel(l < 16, WS_FLAG + ((xcd & 6) * 32 + tq * 16 + l) * FS + 8 * 128 + fg * 8)); };
  auto publish_h2 = [&](int s, u32 last, u32 fv) { gflag(ws, (fv | last) + s * 128, tag); };
  auto finish = [&]() {
  };
  u32 role = wave;
  asm volatile("" : "+s"(role));

  if (role >= 4) {
    const srd_t rwo = __builtin_amdgcn_make_buffer_rsrc((void*)P.wo, 0, 512 * 1024, 0x00020000);
    u32 ha[4];
    u32x4 hl[2][4];
    {
      __builtin_amdgcn_s_setprio(2);
      touch(rx, tx, 0);
      touch(ws, t4, FL + w * 512);
      u64 e8 = ld8h(ws, WS_EPOCH);
      STAMP(43);
      const u32 l = l0, q = l >> 4, tok = l & 15;
      const u32 xo = (T1 - 16 + 4 * w + q) * 1024 + tok * 16;
      u32x4 xr[5][4];
      auto xld = [&](int g) {
        const u32 o = (AB_X34 && g >= 3) ? OOB : xo + g * 16384;
#pragma unroll
        for (int i = 0; i < 4; ++i) xr[g][i] = ld16h(rx, o, 0, i * 256);
      };
      xld(0); xld(1); xld(2);
      __builtin_amdgcn_s_setprio(0);
      STAMP(1);
      sy.cb = opq(L_CNT); sy.one = opq(1);
      f32x4 xn[5][4][2];
      u32x4 mp[4];
      const u32 mo = opq((256 * w + 4 * l) * 4);
#pragma unroll
      for (int g = 0; g < 5; ++g) {
        if (g == 3) { __builtin_amdgcn_s_setprio(2); sy.mod_ld<4>(mp, ws, mo, 0); __builtin_amdgcn_s_setprio(0); STAMP(2); }
        if (g < 4) vmwait<8>(); else vmwait<4>();
        asm volatile("" : "+v"(xr[g][0]), "+v"(xr[g][1]), "+v"(xr[g][2]), "+v"(xr[g][3]));
        if (g == 0) { asm volatile("" : "+v"(e8)); tag = ((u64)rfl((u32)e8) | ((u64)rfl((u32)(e8 >> 32)) << 32)) + 0x40100401ull; }
        float s = 0.f, ss = 0.f;
#pragma unroll
        for (int i = 0; i < 4; ++i)
#pragma unroll
          for (int j = 0; j < 4; ++j) { s = dot2(xr[g][i][j], 0x3f803f80u, s); ss = dot2(xr[g][i][j], xr[g][i][j], ss); }
        s += dpp<0x128>(s); ss += dpp<0x128>(ss);
        s += dpp<0x124>(s); ss += dpp<0x124>(ss);
        s += dpp<0x122>(s); ss += dpp<0x122>(ss);
        s += dpp<0x121>(s); ss += dpp<0x121>(ss);
        const float m = s * (1.f / 512), rs = __builtin_amdgcn_rsqf(fmx(ss * (1.f / 512) - m * m, 0.f) + 1e-5f), nm = -m * rs;
#pragma unroll
        for (int i = 0; i < 4; ++i) {
          xn[g][i][0] = unp4(xr[g][i].x, xr[g][i].y) * rs + nm; xn[g][i][1] = unp4(xr[g][i].z, xr[g][i].w) * rs + nm;
          asm volatile("" : "+v"(xn[g][i][0]), "+v"(xn[g][i][1]));
        }
        SB();
        if (g < 2) { __builtin_amdgcn_s_setprio(2); xld(g + 3); __builtin_amdgcn_s_setprio(0); }
      }
#pragma unroll
      for (int i = 0; i < 4; ++i) ha[i] = opq(((16 * i + tok - 8 * xcd) & 63) * 256 + (((4 * w + q) ^ (tok & 7)) << 4));
      const u32 sba = opq(L_SB + tok * 16), sbw = opq(L_SB + w * 1024 + (l >> 5) * 512 + (l & 1) * 256 + ((l >> 1) & 15) * 16);
      STAMP(24);
      vmwait<0>();
      f32x4 mv = sy.mod_get<4>(mp, ws, mo, 0, tag);
      if (w >= 2) {
#pragma unroll
        for (int j = 0; j < 4; ++j) mv[j] = rbf(adds(mv[j], 1.f));
      }
      lds_st<f32x4>(sbw, mv);
      ARR(cM2); WAIT(cM2, 4);
      STAMP(3);
      f32x4 sh[2][2], sl[2][2];
      auto sbld = [&](int i) {
#pragma unroll
        for (int e = 0; e < 2; ++e) { sh[i & 1][e] = lds_ld<f32x4>(sba + i * 512 + e * 256); sl[i & 1][e] = lds_ld<f32x4>(sba + 2048 + i * 512 + e * 256); }
      };
      u32x4 h0[4];
      sbld(0);
#pragma unroll
      for (int i = 0; i < 4; ++i) {
        if (i < 3) sbld(i + 1);
        SB();
#pragma unroll
        for (int k = 0; k < 5; ++k) {
          const int g = k == 0 ? 1 : k == 1 ? 3 : k == 2 ? 0 : k == 3 ? 2 : 4;
          u32x4 hv = pack8(xn[g][i][0] * sl[i & 1][0] + sh[i & 1][0], xn[g][i][1] * sl[i & 1][1] + sh[i & 1][1]);
          if (g == 1 || g == 2) lds_st<u32x4>(ha[i] + g * 16384, hv);
          else { asm volatile("" : "+v"(hv)); if (g) hl[g - 3][i] = hv; else h0[i] = hv; }
          SB();
        }
      }
      WAIT(cGu, 4);
#pragma unroll
      for (int i = 0; i < 4; ++i) lds_st<u32x4>(ha[i], h0[i]);
      ARR(cH);
      STAMP(4);
    }
    u32x4 W[48];
    u32x2 we[3];
    u64 pf;
    sy.pb = opq(L_CNT + 160 + 4 * w);
    {
      const u32 l = lane_id(), q = l >> 4, tok = l & 15, jj = (l >> 2) & 3, drow = l >> 3, l16 = opq(l * 16);
      const u32 sw = ((l & 7) ^ (drow & 2)) * 16;
      const u32 v0 = opq((16 * w + drow) * 3072 + cg * 128 + sw), v1 = opq((v0 ^ 64) + 8 * 3072 - 1024);
      const u32 ve = opq(drow * 1024 + sw), vo = opq((ve ^ 64) + 7168);
      auto wo_issue = [&](int i) {
        const u32 so = (4 * w + ((i + xcd) & 3)) * 32768 + cg * 128;
        dma4(rwo, L_S + i * 16384 + w * 4096, ve, vo, so, so + 14336);
      };
      const u32 mo = opq((1536 + 256 * w + 4 * l) * 4), xo = opq(q * 12288 + (((w & 1) ? 2560 : 1024) + 64 * cg + 4 * tok) * 4);
      const u32 gfv = opq(WS_FG + (fg * 4 + w) * 128 + tok * 8);
      const u32 wv = opq(wnone ? OOB : l16);
      auto issue = [&](int ks) {
#pragma unroll
        for (int b = 0; b < 3; ++b) W[ks * 3 + b] = ld16h(ws, wv, WS_WG + (ks * 64 + fg * 4 + w) * 3072, b * 1024);
      };
      WAIT(cI4, 4); qkv_issue(5, v0, v1);
      gflag(ws, sel(l + w == 0, WS_HELLO + wg * 8), tag);
      WAIT(cW, 16); qkv_issue(6, v0, v1);
      vmwait<6>(); ARR(cL + 2);
      pf = ld8h(ws, gfv);
      WAIT(cW, 20); qkv_issue(7, v0, v1);
      STAMP(5);
      vmwait<6>(); ARR(cL);
      if (UNLIKELY(!all_eq(pf, tag))) { vmwait<0>(); ARR(cL + 1); sy.poll(ws, gfv, tag, 0xffff); sy.sub<cL + 1>(sy.cb); }
      issue(0); issue(1);
      vmwait<6>(); ARR(cL + 1);
      STAMP(6);
      issue(2);
      WAIT(cWc + 1, 4);
#pragma unroll
      for (int i = 0; i < 4; ++i) { const u32 b = ha[i] + 49152; lds_st<u32x4>(b, hl[0][i]); lds_st<u32x4>(b + 16384, hl[1][i]); }
      ARR(cH + 1);
      u32x4 mp[5];
      sy.mod_ld<5>(mp, ws, mo, xo);
      WAIT(cWc + 3, 4); wo_issue(0); wo_issue(1); wo_issue(2);
      WAIT(cA, 4); wo_issue(3);
      STAMP(7);
      const u32 un = wg + 256 * w, uks = un >> 3, ucg = un & 7;
      const bool uon = un < 344;
      {
        const srd_t rd = __builtin_amdgcn_make_buffer_rsrc((void*)P.wd, 0, 1376 * 1024, 0x00020000);
        const u32 so = uon ? uks * 32768 + ucg * 128 : 0;
        dma4(rd, L_GD + w * 4096, uon ? ve : OOB, uon ? vo : OOB, so, so + 14336);
      }
      issue(3);
      vmwait<23>();
      {
        f32x4 mv = sy.mod_get<5>(mp, ws, mo, xo, tag), gv4;
        if (w >= 2) {
#pragma unroll
          for (int j = 0; j < 4; ++j) mv[j] = rbf(adds(mv[j], 1.f));
        }
        const u32 k = (256 * w + 4 * l) & 511;
        lds_st<u32x2>(L_SB + (w < 2 ? 1024 : 0) + k * 2, pack4(mv));
        if (w >= 2 && (k >> 6) == cg) lds_st<f32x4>(L_G3 + 256 + (k & 63) * 4, mv);
        issue(4);
#pragma unroll
        for (int j = 0; j < 4; ++j) gv4[j] = rbf(fu(opq(uf(sum32(fu(opq(uf(sum16(fu(mp[4][j] & ~1023u))))))))));
        lds_st<f32x4>(L_G3 + (w & 1) * 512 + tok * 16, gv4);
        ARR(cM3);
      }
      vmwait<6>(); ARRP(pWo);
      STAMP(8);
      f32x4 uv[3];
      bool seen = false;
      const u32 sba = opq(L_SB + (l & 4) * 256 + q * 16);
      auto colsum = [&](int k0, int k1) {
        u32x4 sv[4];
#pragma unroll
        for (int k = k0; k < k0 + 4 && k < k1; ++k) sv[k & 3] = lds_ld<u32x4>(sba + k * 64);
        SB();
#pragma unroll
        for (int ks = k0; ks < k1; ++ks) {
          if (ks == 3) vmwait<12>();
          if (ks == 12) vmwait<6>();
          if (ks == 14) {
            vmwait<0>();
            if (w == 0) { seen = all_eq(pf, tag, 0xffff); at8h(pf, ws, FL + 4 * 128 + (l16 >> 1), 0xffff); }
            if (seen) ARR(cXs);
          }
#pragma unroll
          for (int b = 0; b < 3; ++b) mmaz(ks == 0, uv[b], sv[ks & 3], W[ks * 3 + b]);
          SB();
          if (ks + 4 < k1) sv[ks & 3] = lds_ld<u32x4>(sba + (ks + 4) * 64);
          SB();
        }
      };
      {
        const u32 tb = L_GD + w * 4096 + (8 * q + jj) * 128 + (l & 3) * 8 + (((jj >> 1) ^ ((q & 1) << 1)) << 5);
        u32x4 t[4];
#pragma unroll
        for (int cb = 0; cb < 4; ++cb) t[cb] = tr_frag(tb ^ (cb << 5), 512);
#pragma unroll
        for (int cb = 0; cb < 4; ++cb) st16_sc1(t[cb], ws, uon ? l16 : OOB, WS_WD + (uon ? (ucg * 44 + uks) * 4096 : 0) + cb * 1024);
        issue(5);
        const u32 wdf = opq(sel(l == 0 && uon, WS_FD + (ucg * 4 + uks / 11) * 128 + (uks % 11) * 8));
        WAIT(cM3, 4);
        colsum(0, 3);
        vmwait<3>(); gflag(ws, wdf, tag);
        sy.waitz<cPa>(sy.cb);
      }
      STAMP(9);
      u32 pk = 0;
#pragma unroll
      for (int ks = 6; ks < 16; ++ks) {
        if (UNLIKELY(rfl(pk))) sy.waitz<cPa>(sy.cb);
        if (ks < 15) pk = PEEK(cPa);
        issue(ks);
      }
      STAMP(13);
      if (w == 0) at8h(pf, ws, FL + 4 * 128 + (l16 >> 1), 0xffff);
      colsum(3, 16);
      asm volatile("s_nop 7" : "+v"(uv[0]), "+v"(uv[1]), "+v"(uv[2]));
      const u32 l2 = lane_id();
#pragma unroll
      for (int b = 0; b < 3; ++b) {
        const float z = l2 < 32 ? uv[b][0] : 0.f;
        const u32 hh = cvtpk(z, z);
        we[b].x = opq(hh); we[b].y = opq(cvtpk(z - bflo(hh), 0.f));
      }
      if (UNLIKELY(wg == 255 && w == 1)) {
#pragma unroll 1
        for (int i = 0; i < 4; ++i) sy.poll(ws, WS_HELLO + i * 512 + l2 * 8, tag, ~0ull);
        gflag(ws, sel(l2 == 0, WS_EPOCH), tag);
      }
      STAMP(10);
      if (w == 0 && !seen) { vmwait<0>(); if (!all_eq(pf, tag, 0xffff)) sy.poll(ws, FL + 4 * 128 + l2 * 8, tag, 0xffff); ARR(cXs); }
    }
    {
      const u32 l = lane_id(), q = l >> 4, tok = l & 15;
      u32 xa[4];
      const u32 db = opq(L_GD + w * 6144 + l * 8), xv = opq(fg == 15 ? OOB : (l ^ (4 * w)) * 16);
#pragma unroll
      for (int k = 0; k < 4; ++k) xa[k] = opq(L_X0 + tok * 1024 + (((4 * k + q) ^ tok) << 4));
      auto xf = [&](int t, int ks) { return lds_ld<u32x4>(xa[ks & 3] + (ks >> 2) * 256 + t * 16384); };
      f32x4 acc[2][3];
      u32x4 a[4];
      u32x2 te;
      const u32 tea = opq(L_UV + tok * 16 + (q & 1) * 8);
      WAIT(cR, 4);
      WAIT(cXs, 1);
      STAMP(14);
      __builtin_amdgcn_s_setprio(2);
      const u32x4 sp = stats_ld(0, l * 16);
      dma4_sc1(ws, L_X0 + w * 4096, xv, WS_X1 + (T3 + 4 * w) * 1024); dma4_sc1(ws, L_X0 + 16384 + w * 4096, xv, WS_X1 + (T3 + 64 + 4 * w) * 1024);
      ARR(cXi);
      vmwait<8>(); stats(0, sp, l * 16);
      vmwait<4>(); ARR(cXa);
      asm volatile("" ::"v"(pf));
      STAMP(15);
      WAIT(cXa, 4);
      STAMP(16);
#pragma unroll
      for (int n = 0; n < 4; ++n) a[n] = xf(0, n);
#pragma unroll
      for (int s = 0; s < 4; ++s) {
        u32 pk;
        SB();
#pragma unroll
        for (int n = 0; n < 32; ++n) {
          const int t = n >> 4, ks = n & 15, sd = t ? s : s - 1;
          mmaz(ks == 0, acc[t][0], W[ks * 3], a[n & 3]);
          SB();
          auto dump = [&](int b, int hf) {
            const f32x4 v = acc[1 - t][b];
            lds_st<f32x2>(db + (sd & 1) * 24576 + ((1 - t) * 3 + b) * 1024 + hf * 512, hf ? f32x2{v[2], v[3]} : f32x2{v[0], v[1]});
          };
          if (t == 1 && ks >= 2 && ks < 8 && sd < 3) dump((ks - 2) >> 1, ks & 1);
          if (t == 0 && ks >= 1 && ks < 4 && sd >= 0) dump(ks - 1, 0);
          if (n == 4 && s > 0) ARRP(pB);
          if (n == 6) { if (s == 1) ARR(cB); if (s == 3) ARR(cB + 2); }
          if (ks == 10) te = lds_ld<u32x2>(tea + (s * 2 + t) * 256);
          if (n == 12 && s == 2) pk = *(volatile LAS u32*)(sy.pb + 4 * pDf);
          if (n == 5 && s == 0) { vmwait<0>(); ARR(cX); }
          if (n == 9 && s == 0) pk = PEEK(cX);
          if (n == 12 && s == 0) CHECK(pk, cX, 4);
          if (n == 16 && s == 2) { if (UNLIKELY(rfl(pk) < 1)) WAITP(pDf, 1); }
          if (n == 24 && s < 3) pk = *(volatile LAS u32*)(sy.cb + 4 * (cX + 1 + s));
          if (n == 28 && s < 3) { if (UNLIKELY(rfl(pk) < 4)) { if (s == 0) WAIT(cX + 1, 4); if (s == 1) WAIT(cX + 2, 4); if (s == 2) WAIT(cX + 3, 4); } }
          if (n >= 28 && s < 3) xa[n & 3] = opq(xa[n & 3] + (s == 0 ? L_X1 - L_X0 : s == 1 ? L_X2 - L_X1 : L_X0 - L_X2));
          SB();
          mmaz(ks == 0, acc[t][1], W[ks * 3 + 1], a[n & 3]);
          SB();
          if (t == 0 && ks >= 1 && ks < 4 && sd >= 0) dump(ks - 1, 1);
          SB();
          mmaz(ks == 0, acc[t][2], W[ks * 3 + 2], a[n & 3]);
          SB();
          if (n < 28 || s < 3) a[n & 3] = xf(((n + 4) >> 4) & 1, (n + 4) & 15);
          SB();
          if (ks == 15) { mma16(acc[t][0], we[0], te); mma16(acc[t][1], we[1], te); mma16(acc[t][2], we[2], te); SB(); }
        }
        if (s == 0) STAMP(17); else if (s == 1) STAMP(18); else if (s == 2) STAMP(19);
      }
      STAMP(20);
      __builtin_amdgcn_s_setprio(2);
      const u32 l2 = lane_id(), ti = l2 >> 5, bq = (l2 >> 4) & 1, pc = 12 * fg + 3 * w, vb = ti * (4 * H2_RB) + (l2 & 15) * 16;
      const float rs = lds_ld<float>(L_RS + 384 + ti * 64 + (l2 & 15) * 4);
      asm volatile("s_nop 4" : "+v"(acc[0][0]), "+v"(acc[0][1]), "+v"(acc[0][2]), "+v"(acc[1][0]), "+v"(acc[1][1]), "+v"(acc[1][2]));
      const u32 so = WS_H2 + ((T3 >> 4) + 3) * H2_RB, hv0 = sel(pc + bq < 172, vb + (pc + bq) * 256), hv1 = sel(bq == 0 && pc + 2 < 172, vb + (pc + 2) * 256);
      u32x2 h[3];
#pragma unroll
      for (int b = 0; b < 3; ++b) {
        f32x4 g, u;
#pragma unroll
        for (int k = 0; k < 4; ++k) { auto t = __builtin_amdgcn_permlane32_swap(uf(acc[0][b][k]), uf(acc[1][b][k]), false, false); g[k] = fu(t[0]); u[k] = fu(t[1]); }
        h[b] = glu2(glu1(g, rs * -LOG2E), u, rs);
        if (b >= 1) {
          auto ex = __builtin_amdgcn_permlane16_swap(h[b & 2].x, h[b].x, false, false);
          auto ey = __builtin_amdgcn_permlane16_swap(h[b & 2].y, h[b].y, false, false);
          st16_sc1(u32x4{ex[0], ey[0], ex[1], ey[1]}, ws, b == 1 ? hv0 : hv1, so);
        }
      }
      STAMP(21);
      const u32 hfv = h2_flagoff(), pv = FL + 10 * 128 + l2 * 8;
      vmwait<0>();
      publish_h2(3, IFLAST(cPh + 3), hfv);
      __builtin_amdgcn_s_setprio(0);
      STAMP(22);
      if (w == 0) {
        u64 pf = 0;
        do { at8h(pf, ws, pv, 0x7fff7fff); vmwait<0>(); sy.n -= 8; } while (!all_eq(pf, tag, 0x7fff) && sy.n > 0);
        ARR(cF + 1);
        STAMP(37);
        if (!all_eq(pf, tag, 0x7fff0000)) sy.poll(ws, pv, tag, 0x7fff0000);
        ARR(cF + 1);
        STAMP(38);
      }
      if (w >= 2) {
        const srd_t ro = __builtin_amdgcn_make_buffer_rsrc(P.out, 0, 2048 * 1024, 0x00020000);
        const u32 c8 = l2 & 7, rtk = 8 * (w & 1) + (l2 >> 3), ov = opq((l2 >> 3) * 1024 + c8 * 16);
        const u32 r0 = rtk * 256 + (((2 * c8) ^ (rtk & 7)) << 4);
        const f32x4 gb0 = lds_ld<f32x4>(L_G3 + 512 + c8 * 32), gb1 = lds_ld<f32x4>(L_G3 + 528 + c8 * 32);
        u32 cd = sy.cb;
#pragma unroll 1
        for (u32 g = 0; g < 4; ++g) {
          const u32 wr = 2 * (g & 1) + (w & 1);
          const u32 ra = opq(r0 + (g < 2 ? L_X1 : L_X2) + (g & 1) * 16384), rb = opq(ra ^ 16), xa = opq((g < 2 ? L_XR + wr * 2048 : L_GD + wr * 6144) + l2 * 16);
          sy.wait<cD>(cd, 4);
          f32x4 y[8];
#pragma unroll
          for (int k = 0; k < 8; ++k) y[k] = lds_ld<f32x4>((k < 4 ? ra : rb) + (k & 3) * 4096);
          const f32x4 x0 = lds_ld<f32x4>(xa), x1 = lds_ld<f32x4>(xa + 1024);
          SB();
          st16(pack8(resid4(y[0], y[1], y[2], y[3], x0, gb0), resid4(y[4], y[5], y[6], y[7], x1, gb1)), ro, ov, (T1 + 16 * g + 8 * (w & 1)) * 1024 + cg * 128);
          cd += 4;
        }
      }
      STAMP(23);
    }
#undef hv0
#undef hv1
    finish();
    asm volatile("s_endpgm" : "=v"(wm[0]), "=v"(wm[1]), "=v"(wm[2]), "=v"(wm[3]), "=v"(cv), "=v"(ep), "=v"(mv0) :: "memory");
  }
  {
    u32x4 W[48];
    f32x4 x1r[2];
    sy.cb = opq(L_CNT); sy.one = opq(1); sy.pb = opq(L_CNT + 160 + 4 * w);
    u64 pf;
    u32 pv, dfv;
    auto wdl = [&](int n, u32 l16) {
      if (n < 44) W[n] = ld16h(ws, (n >= 40 && w == 3) ? OOB : l16, WS_WD + (cg * 44 + 11 * w + (n >> 2)) * 4096, (n & 3) * 1024);
    };
    const u32 kc = (xcd >> 1) * 4 + tq;
    {
      const u32 l = lane_id(), q = l >> 4, tok = l & 15, jj = (l >> 2) & 3, drow = l >> 3;
      STAMP(32);
      const u32 v0 = opq((16 * w + drow) * 3072 + cg * 128 + ((l & 7) ^ (drow & 2)) * 16), v1 = opq((v0 ^ 64) + 8 * 3072 - 1024);
      const u32 pa = opq(L_RS + w * 256 + ((l & 7) * 8 + (l >> 5) * 4 + (q & 1) * 2) * 4), po = opq(L_RS + tok * 16), pvm = opq(sel(l < 16, l * 16));
      vmwait<4>();
      asm volatile("" : "+v"(cv), "+v"(ep));
      const float sc[4] = {bflo((u32)cv), bfhi((u32)cv), bflo((u32)(cv >> 32)), bfhi((u32)(cv >> 32))};
      u32 slo[4], shi[4];
#pragma unroll
      for (int i = 0; i < 4; ++i) {
        const float s = sc[i] * __builtin_amdgcn_rcpf(1.f + __builtin_amdgcn_exp2f(sc[i] * -LOG2E));
        slo[i] = opq(cvtpk(s, 0.f)); shi[i] = opq(slo[i] << 16);
      }
      tag = ((u64)rfl((u32)ep) | ((u64)rfl((u32)(ep >> 32)) << 32)) + 0x40100401ull;
      asm volatile("" ::"s"(tag));
      auto mod_gemv = [&]() {
        asm volatile("" : "+v"(wm[0]), "+v"(wm[1]), "+v"(wm[2]), "+v"(wm[3]));
        float a[8] = {}, b[4], d[2];
#pragma unroll
        for (int i = 0; i < 4; ++i)
#pragma unroll
          for (int j = 0; j < 4; ++j) { a[2 * j] = dot2(wm[i][j], slo[i], a[2 * j]); a[2 * j + 1] = dot2(wm[i][j], shi[i], a[2 * j + 1]); }
#pragma unroll
        for (int j = 0; j < 4; ++j) { auto t = __builtin_amdgcn_permlane32_swap(uf(a[j]), uf(a[j + 4]), false, false); b[j] = fu(t[0]) + fu(t[1]); }
#pragma unroll
        for (int j = 0; j < 2; ++j) { auto t = __builtin_amdgcn_permlane16_swap(uf(b[j]), uf(b[j + 2]), false, false); d[j] = fu(t[0]) + fu(t[1]); }
        d[0] += dpp<0x128>(d[0]); d[1] += dpp<0x128>(d[1]);
        lds_st<f32x2>(pa, f32x2{d[0], d[1]});
        if (LAST(cM1)) {
          const f32x4 s = (lds_ld<f32x4>(po) + lds_ld<f32x4>(po + 256)) + (lds_ld<f32x4>(po + 512) + lds_ld<f32x4>(po + 768));
          u32x4 e;
#pragma unroll
          for (int j = 0; j < 4; ++j) e[j] = ((uf(s[j]) + 512u) & ~1023u) | ((u32)(tag >> (10 * j)) & 1023u);
          st16_sc1(e, ws, pvm, WS_MODP + kq * 12288 + lc * 256);
        }
      };
      SB();
      vmwait<0>();
      if (kq < 4 && lc < 16) { __builtin_amdgcn_s_setprio(3); mod_gemv(); __builtin_amdgcn_s_setprio(0); STAMP(34); }
      touch(rqkv, sel(l < 14 && ci != 7, (56 * cj + 14 * w + l) * 1024 + cs * 128), 0);
      qkv_issue(0, v0, v1);
      {
        const srd_t rg = __builtin_amdgcn_make_buffer_rsrc((void*)P.wg, 0, 512 * 2752, 0x00020000);
        const srd_t ru = __builtin_amdgcn_make_buffer_rsrc((void*)P.wu, 0, 512 * 2752, 0x00020000);
        const u32 row = 8 * w + q, o = (fg == 15 || AB_GU) ? OOB : (2752 * (row + 32 * kc) + 192 * fg) & ~127u;
        const u32 sl = tok ^ (((row ^ fg) & 1) | (row & 2) | ((row >> 1) & 4)), e14 = fg == 14 ? OOB : 0;
        const u32 g0 = (o + (sl << 4)) | (e14 & (sl << 28)), g1 = (o + ((sl ^ 8) << 4)) | (e14 & ~(sl << 28));
        dma2(rg, L_H + w * 2048, g0, g0 + 11008 - 1024, 0);
        qkv_issue(1, v0, v1);
        dma2(ru, L_H + 8192 + w * 2048, g1, g1 + 11008 - 1024, 0);
      }
      u32 tb = opq(L_Q + (8 * q + jj) * 128 + (l & 3) * 8 + ((w ^ (jj >> 1) ^ ((q & 1) << 1)) << 5));
      auto rd1 = [&](int c, int f) { W[6 * c + f] = tr_frag(tb + (c & 1) * 24576 + (f % 3) * 8192 + (f / 3) * 4096, 512); };
      auto rd = [&](int c) {
#pragma unroll
        for (int f = 0; f < 6; ++f) rd1(c, f);
      };
      u32 pk;
      qkv_seg(2, 0, v0, v1); vmwait<12>(); ARR(cW);
      qkv_seg(2, 1, v0, v1); pk = PEEK(cW);
      qkv_seg(2, 2, v0, v1); CHECK(pk, cW, 4);
      STAMP(1);
      {
        const u32 mr = (lc < 16 || AB_MR) ? OOB : mv0;
        rd1(0, 0); rd1(0, 1); rd1(0, 2);
        wm[0] = ld16h(rm, mr, 0, 0); wm[1] = ld16h(rm, mr, 6144, 0);
        rd1(0, 3); rd1(0, 4); rd1(0, 5);
        vmwait<8>(); ARR(cW);
        wm[2] = ld16h(rm, mr, 12288, 0); wm[3] = ld16h(rm, mr, 18432, 0);
      }
      pk = PEEK(cW);
      {
        const u32 m = (l >> 1) & 1, pr = (jj ^ fg) & 1, gx = pr | (jj & 2) | ((q & 1) << 2) | (m << 3);
        u32 ga[3];
#pragma unroll
        for (int b = 0; b < 3; ++b) ga[b] = opq(L_H + m * 8192 + (8 * q + jj) * 256 + (((3 * w + b + 4 * pr) ^ gx) << 4) + (l & 1) * 8);
        qkv_seg(3, 0, v0, v1); CHECK(pk, cW, 8);
        u32x4 t[3];
#pragma unroll
        for (int b = 0; b < 3; ++b) t[b] = tr_frag(ga[b], 1024);
        ARR(cGu);
        STAMP(33);
        qkv_seg(3, 1, v0, v1);
        const u32 sv = (wnone || AB_GU) ? OOB : l * 16;
#pragma unroll
        for (int b = 0; b < 3; ++b) st16_sc1(t[b], ws, sv, WS_WG + (kc * 64 + fg * 4 + w) * 3072 + b * 1024);
      }
      rd1(1, 0); rd1(1, 1); rd1(1, 2);
      qkv_seg(3, 2, v0, v1);
      rd1(1, 3); rd1(1, 4); rd1(1, 5); tb = opq(tb + 49152);
      qkv_seg(4, 0, v0, v1); vmwait<11>(); ARR(cW);
      touch(__builtin_amdgcn_make_buffer_rsrc((void*)P.wo, 0, 512 * 1024, 0x00020000),
            sel(l < 5 && ci != 7, (20 * cj + 5 * w + l) * 1024 + ((3 - ((u32)(size_t)P.wo >> 7)) & 7) * 128), 0);
      qkv_seg(4, 1, v0, v1); pk = PEEK(cW);
      qkv_seg(4, 2, v0, v1); ARR(cI4); CHECK(pk, cW, 12);
      rd(2);
      vmwait<7>(); ARR(cW);
      gflag(ws, sel(l == 0, WS_FG + (fg * 4 + w) * 128 + kc * 8), tag);
      if (kq < 4 && lc >= 16) mod_gemv();
      STAMP(2);
      u32 e0 = opq(q * 256 + ((tok ^ q) << 4)), o0 = opq((e0 ^ 64) + 1024);
      auto hf = [&](int rb, int ks) { return lds_ld<u32x4>(((ks & 1) ? o0 : e0) + rb * 16384 + (ks >> 1) * 2048); };
      u32 sp0, sp1, sc0, sc1;
      {
        const bool par = __builtin_amdgcn_inverse_ballot_w64(lane_mask(0)), c0 = __builtin_amdgcn_inverse_ballot_w64(lane_mask(1)), c1 = __builtin_amdgcn_inverse_ballot_w64(lane_mask(2));
        const u32 s0 = par ? 0x01000c0cu : 0x0c0c0100u, s1 = par ? 0x03020c0cu : 0x0c0c0302u, zz = 0x0c0c0c0cu;
        sp0 = opq(c0 ? zz : s0); sp1 = opq(c1 ? zz : s1); sc0 = opq(c0 ? s0 : zz); sc1 = opq(c1 ? s1 : zz);
      }
      WAIT(cH, 4);
      STAMP(3);
      u32x4 a[4];
      u32x2 qb[2], kb[3], vb[3];
      {
        f32x4 aq[2], ak[3], av[3];
#pragma unroll
        for (int n = 0; n < 4; ++n) a[n] = hf(n % 3, n / 3);
        SB();
#pragma unroll
        for (int n = 0; n < 48; ++n) {
          const int ks = n / 3, rb = n % 3;
          if (n == 42) { WAIT(cL + 1, 4); rd(7); STAMP(4); SB(); }
          mmaz(ks == 0, ak[rb], W[ks * 3 + 1], a[n & 3]);
          SB();
          if (n == 0) pk = PEEK(cW);
          if (n == 2) CHECK(pk, cW, 16);
          if (n >= 2 && n < 8) rd1(3, n - 2);
          if (n == 8) { tb = opq(tb - 49152); vmwait<1>(); ARR(cW); }
          if (n == 11) pk = PEEK(cW);
          if (n == 13) CHECK(pk, cW, 20);
          if (n >= 13 && n < 19) rd1(4, n - 13);
          if (n == 19) ARR(cWc + 1);
          if (n == 22) pk = PEEK(cL + 2);
          if (n == 24) CHECK(pk, cL + 2, 4);
          if (n >= 24 && n < 30) rd1(5, n - 24);
          if (n == 30) { pk = PEEK(cL); tb = opq(tb + 49152); }
          if (n == 32) CHECK(pk, cL, 4);
          if (n >= 32 && n < 38) rd1(6, n - 32);
          if (n == 38) ARR(cWc + 3);
          SB();
          if (rb) mmaz(ks == 0, aq[rb - 1], W[ks * 3], a[n & 3]);
          mmaz(ks == 0, av[rb], a[n & 3], W[ks * 3 + 2]);
          SB();
          if (n + 4 < 48) a[n & 3] = hf((n + 4) % 3, (n + 4) / 3);
          SB();
        }
        ARR(cA);
        asm volatile("s_nop 4" : "+v"(aq[0]), "+v"(aq[1]), "+v"(ak[0]), "+v"(ak[1]), "+v"(ak[2]), "+v"(av[0]), "+v"(av[1]), "+v"(av[2]));
#pragma unroll
        for (int k = 0; k < 3; ++k) { kb[k] = pack4(ak[k]); vb[k] = pack4(av[k]); }
        qb[0] = pack4(aq[0]); qb[1] = pack4(aq[1]);
      }
      auto attn = [&](int j) {
        const bool par = __builtin_amdgcn_inverse_ballot_w64(lane_mask(0)), c0 = __builtin_amdgcn_inverse_ballot_w64(lane_mask(1)), c1 = __builtin_amdgcn_inverse_ballot_w64(lane_mask(2));
        const float cs = 0.25f * LOG2E;
        float e0[2], e1[2], p0[2], p1[2];
#pragma unroll
        for (int t = 0; t < 2; ++t) {
          const float ni = (j == 0 && t == 0 && T1 == 0) ? -__builtin_inff() : 0.f;
          const f32x4 sc = mfma16(kb[t + 1], qb[t], z4), sp = mfma16(kb[t], qb[t], f32x4{ni, ni, ni, ni});
          e0[t] = par ? (c0 ? sc[1] : sp[1]) : (c0 ? sc[0] : sp[0]);
          e1[t] = par ? (c1 ? sc[3] : sp[3]) : (c1 ? sc[2] : sp[2]);
        }
        auto m2 = __builtin_amdgcn_permlane32_swap(uf(fmx(e0[0], e1[0])), uf(fmx(e0[1], e1[1])), false, false);
        const float mh = max16(fmx(fu(m2[0]), fu(m2[1]))) * cs;
        auto mx = __builtin_amdgcn_permlane32_swap(uf(mh), uf(mh), false, false);
        f32x4 o[2];
#pragma unroll
        for (int t = 0; t < 2; ++t) {
          p0[t] = __builtin_amdgcn_exp2f(__builtin_fmaf(e0[t], cs, -fu(mx[t]))); p1[t] = __builtin_amdgcn_exp2f(__builtin_fmaf(e1[t], cs, -fu(mx[t])));
          const u32 pk = cvtpk(p0[t], p1[t]);
          const u32x4 pb = {__builtin_amdgcn_perm(0u, pk, sp0), __builtin_amdgcn_perm(0u, pk, sp1), __builtin_amdgcn_perm(0u, pk, sc0), __builtin_amdgcn_perm(0u, pk, sc1)};
          o[t] = mfma32(u32x4{vb[t].x, vb[t].y, vb[t + 1].x, vb[t + 1].y}, pb, z4);
        }
        auto s2 = __builtin_amdgcn_permlane32_swap(uf(p0[0] + p1[0]), uf(p0[1] + p1[1]), false, false);
        const float ih = __builtin_amdgcn_rcpf(sum16(fu(s2[0]) + fu(s2[1])));
        auto iv = __builtin_amdgcn_permlane32_swap(uf(ih), uf(ih), false, false);
        const u32x2 oa = pack4(o[0] * fu(iv[0])), ob = pack4(o[1] * fu(iv[1]));
        auto ex = __builtin_amdgcn_permlane16_swap(oa.x, ob.x, false, false);
        auto ey = __builtin_amdgcn_permlane16_swap(oa.y, ob.y, false, false);
        return u32x4{ex[0], ey[0], ex[1], ey[1]};
      };
      auto o_st = [&](int j, u32x4 v) {
        const u32 l = lane_id();
        st16_sc1(v, ws, (l & 16) * 1024 + (l & 32) * 8 + (l & 15) * 16, WS_O + ((T1 >> 4) + 2 * j) * 16384 + (4 * cg + w) * 512);
      };
      STAMP(5);
      o_st(0, attn(0));
      STAMP(6);
      e0 += 49152; o0 += 49152;
      WAIT(cH + 1, 4);
#pragma unroll
      for (int n = 0; n < 4; ++n) a[n] = hf(n & 1, n >> 1);
      SB();
      const u32 l3 = lane_id();
      const u32 ofv = opq(sel(l3 < 8, WS_FLAG + (xcd * 32 + tq * 8 + l3) * FS + (cg >> 1) * 128 + ((cg & 1) * 4 + w) * 8));
      const u32 otv = opq(sel(l3 < 8, (l3 >> 2) * 16384 + (l3 & 3) * 128));
      pv = opq(FL + w * 128 + l3 * 8);
      auto publish_o = [&](int j) {
        gflag(ws, ofv + j * 64, tag);
        retouch(ws, otv, WS_O + ((T1 >> 4) + 2 * j) * 16384 + (4 * cg + w) * 512);
      };
      {
        f32x4 aq[2], ak[2], av[2];
        SB();
#pragma unroll
        for (int n = 0; n < 32; ++n) {
          const int ks = n >> 1, rb = n & 1;
          if (n == 16) { vmwait<0>(); publish_o(0); STAMP(27); SB(); }
          if (n == PAUSEAT) { ARR(cPa); SB(); }
          mmaz(ks == 0, aq[rb], W[ks * 3], a[n & 3]); mmaz(ks == 0, ak[rb], W[ks * 3 + 1], a[n & 3]); mmaz(ks == 0, av[rb], a[n & 3], W[ks * 3 + 2]);
          if (n + 4 < 32) a[n & 3] = hf(n & 1, (n + 4) >> 1);
          SB();
        }
        pf = 0; at8h(pf, ws, pv, 0xff);
        asm volatile("s_nop 6" : "+v"(aq[0]), "+v"(aq[1]), "+v"(ak[0]), "+v"(ak[1]), "+v"(av[0]), "+v"(av[1]));
        kb[0] = kb[2]; vb[0] = vb[2];
#pragma unroll
        for (int k = 0; k < 2; ++k) { kb[k + 1] = pack4(ak[k]); vb[k + 1] = pack4(av[k]); qb[k] = pack4(aq[k]); }
      }
      STAMP(35);
      o_st(1, attn(1));
      STAMP(36);
      {
      const u32 l = lane_id(), q = l >> 4, tok = l & 15, jj = (l >> 2) & 3, l16 = opq(l * 16);
      u32x4 a[2][4], xres[2];
      auto o_ld = [&](int j) {
#pragma unroll
        for (int rb = 0; rb < 2; ++rb)
#pragma unroll
          for (int i = 0; i < 4; ++i) a[rb][i] = ld16h(ws, l16, WS_O + ((T1 >> 4) + 2 * j + rb) * 16384 + (4 * w + ((i + xcd) & 3)) * 1024, 0);
      };
      vmwait<1>();
      if (UNLIKELY(!all_eq(pf, tag, 0xff))) sy.poll(ws, pv, tag, 0xff);
      STAMP(8);
      o_ld(0);
      const u32 c8 = l & 7, rtk = 8 * (w & 1) + (l >> 3);
      const u32 ev = opq((8 * w + (l >> 3)) * 1024 + c8 * 16);
#pragma unroll
      for (int k = 0; k < 2; ++k) xres[k] = ld16h(rx, ev, (T1 + 32 * k) * 1024 + cg * 128, 0);
      sy.sub<cPa>(sy.cb);
      const u32 tb = L_S + w * 4096 + (8 * q + jj) * 128 + (l & 3) * 8 + (((jj >> 1) ^ ((q & 1) << 1)) << 5);
      WAITP(pWo, 1);
#pragma unroll
      for (int cb = 0; cb < 4; ++cb) {
        const u32 b = opq(tb ^ (cb << 5));
#pragma unroll
        for (int i = 0; i < 4; ++i) W[i * 4 + cb] = tr_frag(b + i * 16384, 512);
      }
      ARR(cR);
      const u32 wa = opq(L_S + w * 4096 + tok * 256 + ((q ^ (tok & 7)) << 4)), wb = opq(wa ^ 64);
      const u32 r0 = opq(L_S + (w >> 1) * 16384 + rtk * 256 + (((2 * c8) ^ (rtk & 7)) << 4)), r1 = opq(r0 ^ 16);
      const u32 sv = opq(sel(c8 == 0, rtk * 8)), g3 = opq(L_G3 + c8 * 32);
      const u32 fv = opq(sel(l < 16, WS_FLAG + (((xcd & 6) + (l & 1)) * 32 + ((xcd & 1) * 2 + (r >> 4)) * 8 + (l >> 1)) * FS + 4 * 128 + (r & 15) * 8));
      vmwait<10>(); publish_o(1);
      STAMP(7);
#pragma unroll
      for (int j = 0; j < 2; ++j) {
        u32 pk = PEEK(cR), pm;
        if (j == 0) pm = PEEK(cM3);
        SB();
        f32x4 acc[2][4];
#pragma unroll
        for (int rb = 0; rb < 2; ++rb)
#pragma unroll
          for (int i = 0; i < 4; ++i) {
            switch ((j == 0 ? 11 : 10) - 4 * rb - i) {
              case 1: vmwait<1>(); break; case 2: vmwait<2>(); break; case 3: vmwait<3>(); break; case 4: vmwait<4>(); break; case 5: vmwait<5>(); break; case 6: vmwait<6>(); break; case 7: vmwait<7>(); break;
              case 8: vmwait<8>(); break; case 9: vmwait<9>(); break; case 10: vmwait<10>(); break; default: vmwait<11>(); break;
            }
            if (rb == 1 && i == 0) { if (j == 0) CHECK(pk, cR, 4); else CHECK(pk, cR, 12); }
#pragma unroll
            for (int cb = 0; cb < 4; ++cb) {
              mmaz(i == 0, acc[rb][cb], W[i * 4 + cb], a[rb][i]);
              if (rb == 1 && i >= 2 && !(cb & 1)) { const int k = 2 * (i - 2) + (cb >> 1); SB(); lds_st<f32x4>(((k & 1) ? wb : wa) + (k >> 1) * 128, acc[0][k]); SB(); }
            }
          }
        if (j == 0) CHECK(pm, cM3, 4);
        asm volatile("s_nop 4" : "+v"(acc[1][0]), "+v"(acc[1][1]), "+v"(acc[1][2]), "+v"(acc[1][3]) :: "memory");
        lds_st<f32x4>(wa + 16384, acc[1][0]); SB(); lds_st<f32x4>(wb + 16384, acc[1][1]); SB(); lds_st<f32x4>(wa + 16512, acc[1][2]); SB(); lds_st<f32x4>(wb + 16512, acc[1][3]);
        ARR(cR);
        const f32x4 ga0 = lds_ld<f32x4>(g3), ga1 = lds_ld<f32x4>(g3 + 16), s0 = lds_ld<f32x4>(g3 + 256), s1 = lds_ld<f32x4>(g3 + 272);
        if (j == 0) { vmwait<2>(); asm volatile("" : "+v"(xres[0]), "+v"(xres[1])); at8h(pf, ws, pv + 64, 0xff); }
        if (j == 1) {
          vmwait<2>();
          if (UNLIKELY(!all_eq(pf, tag))) sy.poll(ws, dfv, tag, 0x7ff);
#pragma unroll
          for (int n = 0; n < EW1; ++n) wdl(n, l16);
        }
        f32x4 xa = unp4(xres[j].x, xres[j].y), xb = unp4(xres[j].z, xres[j].w);
        asm volatile("" : "+v"(xa), "+v"(xb));
        if (j == 0) STAMP(9); else STAMP(13);
        WAIT(cR, 8 + 8 * j);
        f32x4 y[8];
#pragma unroll
        for (int k = 0; k < 8; ++k) y[k] = lds_ld<f32x4>((k < 4 ? r0 : r1) + (k & 3) * 4096);
        ARR(cR);
        const f32x4 u0 = resid4(y[0], y[1], y[2], y[3], xa, ga0), u1 = resid4(y[4], y[5], y[6], y[7], xb, ga1);
        const u32x2 m0 = scale4(u0, s0), m1 = scale4(u1, s1);
        st16_sc1(u32x4{m0.x, m0.y, m1.x, m1.y}, ws, ev, WS_X1 + (T1 + 32 * j) * 1024 + cg * 128);
        float sm, sq;
        mom8(u0, u1, sm, sq);
        sm += dpp<0xB1>(sm); sq += dpp<0xB1>(sq);
        sm += dpp<0x4E>(sm); sq += dpp<0x4E>(sq);
        sm += dpp<0x141>(sm); sq += dpp<0x141>(sq);
        __builtin_amdgcn_raw_buffer_store_b64(u32x2{uf(sm), uf(sq)}, ws, sv, WS_STAT + (((T1 >> 4) + 2 * j + (w >> 1)) * 8 + cg) * 128, SC1);
        if (j == 0) { lds_st<f32x4>(L_XR + w * 2048 + l16, u0); lds_st<f32x4>(L_XR + w * 2048 + 1024 + l16, u1); }
        else { x1r[0] = u0; x1r[1] = u1; }
        if (j == 0) STAMP(10); else STAMP(14);
        u32 pend = 0;
        if (j == 0) {
          vmwait<2>();
          if (UNLIKELY(!all_eq(pf, tag, 0xff))) {
            at8h(pf, ws, pv + 64, 0xff);
            vmwait<1>(); gflag(ws, fv | IFLAST(cPx), tag); pend = OOB;
            vmwait<1>();
            if (!all_eq(pf, tag, 0xff)) sy.poll(ws, pv + 64, tag, 0xff);
          }
          STAMP(12);
          o_ld(1);
          dfv = opq(WS_FD + (cg * 4 + w) * 128 + (l < (w == 3 ? 10 : 11) ? l * 8 : 0));
          pf = ld8h(ws, dfv);
          vmwait<9>();
          if (!pend) pend = IFLAST(cPx);
        } else {
#pragma unroll
          for (int n = EW1; n < EW2; ++n) wdl(n, l16);
          vmwait<EW2 - EW1>(); pend = IFLAST(cPx + 1);
        }
        gflag(ws, (fv | pend) + j * 128, tag);
        retouch(ws, (sv & OOB) | ev, WS_X1 + (T1 + 32 * j) * 1024 + cg * 128);
        if (j == 0) STAMP(11); else STAMP(15);
      }
      }
    }
    {
      const u32 l16 = opq(lane_id() * 16);
      auto wd = [&](int n) { wdl(n, l16); };
      u32 d01, d2, sta, hv0, hv1;
      {
        const u32 l = lane_id(), ti = l >> 5, bq = (l >> 4) & 1, tok = l & 15, db = L_GD + w * 6144 + ti * 3072 + tok * 8;
        const u32 pc = 12 * fg + 3 * w, vb = ti * (4 * H2_RB) + tok * 16;
        d01 = opq(db + bq * 1024); d2 = opq(db + 2048 + bq * 128);
        sta = opq(L_RS + ti * 64 + tok * 4);
        hv0 = opq(sel(pc + bq < 172, vb + (pc + bq) * 256)); hv1 = opq(sel(bq == 0 && pc + 2 < 172, vb + (pc + 2) * 256));
      }
      const u32 hfv = h2_flagoff();
      auto quad = [&](u32 a) { const f32x2 x = lds_ld<f32x2>(a), y = lds_ld<f32x2>(a + 512); return f32x4{x[0], x[1], y[0], y[1]}; };
      auto epilogue = [&](int s, auto slot) {
        const u32 o = (s & 1) * 24576, so = WS_H2 + ((T3 >> 4) + s) * H2_RB;
        const float rs = lds_ld<float>(sta + s * 128);
        f32x4 g[3], u[3];
#pragma unroll
        for (int k = 0; k < 3; ++k) { const u32 da = k < 2 ? d01 + o + 128 * k : d2 + o; g[k] = quad(da); u[k] = quad(da + 256); }
        if (s == 0) ARRP(pDf);
        slot(0); slot(1); SB();
        const float rn = rs * -LOG2E;
        u32x2 h[3];
#pragma unroll
        for (int k = 0; k < 3; ++k) {
          if (k) { slot(4 * k); slot(4 * k + 1); SB(); }
          const F4 e = glu1(g[k], rn);
          slot(4 * k + 2); slot(4 * k + 3); SB();
          h[k] = glu2(e, u[k], rs);
          if (k == 1) st16_sc1(u32x4{h[0].x, h[0].y, h[1].x, h[1].y}, ws, hv0, so);
          SB();
        }
        auto ex = __builtin_amdgcn_permlane16_swap(h[2].x, h[2].x, false, false);
        auto ey = __builtin_amdgcn_permlane16_swap(h[2].y, h[2].y, false, false);
        st16_sc1(u32x4{ex[0], ey[0], ex[1], ey[1]}, ws, hv1, so);
      };
      const u32 xv = opq(fg == 15 ? OOB : l16 ^ (64 * w));
      auto x1_row = [&](int s, int i) {
        dma1_sc1(ws, (s == 1 ? L_X1 : s == 2 ? L_X2 : L_X0) + (i >> 2) * 16384 + w * 4096 + (i & 3) * 1024, xv ^ ((i & 3) << 4),
                 WS_X1 + (T3 + 64 * (i >> 2) + 16 * s + 4 * w + (i & 3)) * 1024);
      };
      WAIT(cR, 20);
      WAIT(cXi, 4);
      STAMP(16);
#pragma unroll
      for (int i = 0; i < 8; ++i) x1_row(1, i);
      if (w == 0) at8h(pf, ws, FL + 5 * 128 + (l16 >> 1), 0xffff);
#pragma unroll
      for (int n = EW2; n < 16; ++n) wd(n);
      vmwait<16 - EW2>(); ARR(cX + 1);
      if (w == 0) { if (!all_eq(pf, tag, 0xffff)) sy.poll(ws, FL + 5 * 128 + (l16 >> 1), tag, 0xffff); ARR(cXs + 1); } else WAIT(cXs + 1, 1);
      STAMP(37);
      const u32x4 sp = stats_ld(1, l16);
#pragma unroll
      for (int i = 0; i < 8; ++i) x1_row(2, i);
#pragma unroll
      for (int n = 16; n < 20; ++n) wd(n);
      vmwait<12>(); stats(1, sp, l16);
      vmwait<4>(); ARR(cX + 2);
      STAMP(17);
      WAITP(pB, 1); STAMP(18);
      u32 ar;
      epilogue(0, [&](int i) { if (i == 2) ar = PEEK(cB); if (i == 4) CHECK(ar, cB, 4); if (i >= 4) x1_row(3, i - 4); else wd(20 + i); });
#pragma unroll
      for (int n = 24; n < 28; ++n) wd(n);
      STAMP(19);
      WAITP(pB, 2); STAMP(20);
      epilogue(1, [&](int i) {
        if (i == 2) { vmwait<6>(); ARR(cX + 3); ar = sy.add_go<cPh>(sy.cb); }
        if (i == 4) publish_h2(0, sy.add_get(ar), hfv);
        wd(28 + i);
      });
#pragma unroll
      for (int n = 40; n < 44; ++n) wd(n);
      STAMP(21);
      WAITP(pB, 3); STAMP(22);
      epilogue(2, [&](int i) {
        if (i == 2) { STAMP(40); vmwait<4>(); STAMP(41); ar = sy.add_go<cPh + 1>(sy.cb); }
        if (i == 4) { publish_h2(1, sy.add_get(ar), hfv); if (w == 0) at8h(pf, ws, FL + 8 * 128 + (l16 >> 1), 0x7fff); }
      });
      STAMP(23);
      const u32 l = lane_id();
      lds_st<f32x4>(L_GD + w * 6144 + l16, x1r[0]); lds_st<f32x4>(L_GD + w * 6144 + 1024 + l16, x1r[1]);
      if (w == 0) { vmwait<2>(); if (UNLIKELY(!all_eq(pf, tag, 0x7fff))) sy.poll(ws, FL + 8 * 128 + l * 8, tag, 0x7fff); ARR(cF); } else WAIT(cF, 1);
      STAMP(24);
      u32x4 a[8];
      auto h2_ld = [&](int n) {
        const u32 ks = 11 * w + n % 11;
        a[n & 7] = __builtin_amdgcn_raw_buffer_load_b128(ws, l16, WS_H2 + ((T1 >> 4) + n / 11) * H2_RB + (n % 11 == 10 && ks > 42 ? 42 : ks) * 1024, SC1);
      };
#pragma unroll
      for (int n = 0; n < 8; ++n) h2_ld(n);
      SB();
      WAIT(cB + 2, 4);
      if (w == 0) { at8h(pf, ws, FL + 9 * 128 + l * 8, 0x7fff); vmwait<9>(); } else vmwait<8>();
      STAMP(38);
      publish_h2(2, IFLAST(cPh + 2), hfv);
      STAMP(39);
      u32 wa = opq(L_X1 + w * 4096 + (l & 15) * 256 + (((l >> 4) ^ (l & 7)) << 4)), wb = opq(wa ^ 64);
      f32x4 acc[2][4];
      auto put = [&](int g, int k) { lds_st<f32x4>(((k & 1) ? wb : wa) + (g & 1) * 16384 + (k >> 1) * 128, acc[g & 1][k]); };
      auto done = [&](int g) {
        if (g == 0) ARR(cD); if (g == 1) ARR(cD + 1); if (g == 2) ARR(cD + 2); if (g == 3) ARR(cD + 3);
        if (g == 1) { wa = opq(wa + (L_X2 - L_X1)); wb = opq(wb + (L_X2 - L_X1)); }
      };
#pragma unroll
      for (int g = 0; g < 4; ++g) {
        SB();
#pragma unroll
        for (int i = 0; i < 11; ++i) {
          const int n = 11 * g + i;
          if (n == 3) {
            if (w == 0) { vmwait<4>(); if (UNLIKELY(!all_eq(pf, tag, 0x7fff))) sy.poll(ws, FL + 9 * 128 + (l16 >> 1), tag, 0x7fff); ARR(cF); } else WAIT(cF, 2);
            SB();
          }
          if (n == 14) { WAIT(cF + 1, 1); SB(); }
          if (n == 25) { STAMP(28); WAIT(cF + 1, 2); STAMP(29); SB(); }
          mmaz(i == 0, acc[g & 1][0], W[i * 4], a[n & 7]);
          SB();
          if (g >= 1 && i >= 2 && i < 6) put(g - 1, i - 2);
          if (g >= 1 && i == 6) done(g - 1);
          SB();
#pragma unroll
          for (int cb = 1; cb < 4; ++cb) mmaz(i == 0, acc[g & 1][cb], W[i * 4 + cb], a[n & 7]);
          if (n + 8 < 44) h2_ld(n + 8);
          SB();
        }
        if (g == 0) STAMP(25);
        if (g == 1) STAMP(26);
        if (g == 3) {
          asm volatile("s_nop 4" : "+v"(acc[g & 1][0]), "+v"(acc[g & 1][1]), "+v"(acc[g & 1][2]), "+v"(acc[g & 1][3]));
#pragma unroll
          for (int k = 0; k < 4; ++k) { put(g, k); SB(); }
          done(g);
        }
      }
      STAMP(31);
    }
    finish();
  }
}

extern "C" void kernel_launch(void* const* d_in, const int* in_sizes, int n_in, void* d_out, int out_size, void* d_ws, size_t ws_size,
                              hipStream_t stream) {
  Params p;
  p.x = d_in[0]; p.wqkv = d_in[1]; p.wo = d_in[2]; p.wg = d_in[4]; p.wu = d_in[5]; p.wd = d_in[6]; p.c = d_in[7]; p.wmod = d_in[8];
  p.out = d_out; p.ws = d_ws;
  adaln_block<<<256, 512, 0, stream>>>(p);
}
```
